```python
import torch

def _softmax(x, axis=-1):
    return torch.softmax(x, dim=axis)
def _gelu(x):
    return torch.nn.functional.gelu(x, approximate="tanh")
def _silu(x):
    return torch.nn.functional.silu(x)
def _sigmoid(x):
    return torch.sigmoid(x)
def _layernorm(x, axis=-1, eps=1e-5):
    m = x.mean(dim=axis, keepdim=True)
    v = x.var(dim=axis, keepdim=True, unbiased=False)
    return (x - m) / torch.sqrt(v + eps)
def _scatter_add(tgt, idx, src, axis=0):
    return tgt.clone().index_add_(axis, idx, src)
def _segment_sum(src, seg, n):
    out = torch.zeros((n,) + src.shape[1:], dtype=src.dtype)
    return out.index_add_(0, seg, src)
def _pad(x, pw):
    flat = tuple(v for pair in reversed(pw) for v in pair)
    return torch.nn.functional.pad(x, flat)

kernel_name = 'parallel_sum__sliding_block_cached__geglu_ffn__decode'

def setup_inputs():

    _w = (lambda a: (lambda t: t.to(torch.bfloat16) if t.is_floating_point() else t)(torch.as_tensor(a)))
    def _A_setup():
        B, Sq, D = 4, 1, 640
        Hq, Hkv, Sk, Dh = 32, 32, 8192, 192
        assert Hq % Hkv == 0, (Hq, Hkv)
        return {
            "x": _w((lambda *s: torch.randn(*s, dtype=torch.bfloat16) * 0.05)(B, Sq, D)),
            "k_cache": _w((lambda *s: torch.randn(*s, dtype=torch.bfloat16) * 3.2)(B, Hkv, Sk, Dh)),
            "v_cache": _w((lambda *s: torch.randn(*s, dtype=torch.bfloat16) * 1.6)(B, Hkv, Sk, Dh)),
            "wq": _w((lambda *s: torch.randn(*s, dtype=torch.bfloat16) * 3.2)(D, Hq * Dh) * (D ** -0.5)),
            "wo": _w((lambda *s: torch.randn(*s, dtype=torch.bfloat16) * 1.6)(Hq * Dh, D) * ((Hq * Dh) ** -0.5)),
        }
    _A_in = _A_setup()

    def _B_setup():
        B, S, D, F = 4, 1, 640, 1536
        return {
            "x": _w((lambda *s: torch.randn(*s, dtype=torch.bfloat16) * 0.05)(B, S, D)),
            "w_gate": _w((lambda *s: torch.randn(*s, dtype=torch.bfloat16) * 0.05)(D, F)),
            "w_up": _w((lambda *s: torch.randn(*s, dtype=torch.bfloat16) * 0.05)(D, F)),
            "w_down": _w((lambda *s: torch.randn(*s, dtype=torch.bfloat16) * 0.05)(F, D)),
        }
    _B_in = _B_setup()

    return {
        "A_x": _A_in["x"],
        "A_k_cache": _A_in["k_cache"],
        "A_v_cache": _A_in["v_cache"],
        "A_wq": _A_in["wq"],
        "A_wo": _A_in["wo"],
        "B_x": _B_in["x"],
        "B_w_gate": _B_in["w_gate"],
        "B_w_up": _B_in["w_up"],
        "B_w_down": _B_in["w_down"],
    }

def reference(A_x, A_k_cache, A_v_cache, A_wq, A_wo, B_x, B_w_gate, B_w_up, B_w_down):
    def _A_ref(x, k_cache, v_cache, wq, wo):
        B, Sq, D = x.shape
        Hq, Dh = 32, 192
        Hkv, Sk = k_cache.shape[1], k_cache.shape[2]
        W = min(4096, max(256, Sk // 4))
        rep = Hq // Hkv
        q = (lambda x, *d: x.permute(*d))((x @ wq).reshape(B, Sq, Hq, Dh), 0, 2, 1, 3)
        k = (lambda x, n, axis: torch.repeat_interleave(x, n, dim=axis))(k_cache[:, :, Sk - W:, :], rep, axis=1)
        v = (lambda x, n, axis: torch.repeat_interleave(x, n, dim=axis))(v_cache[:, :, Sk - W:, :], rep, axis=1)
        scores = torch.einsum("bhqd,bhkd->bhqk", q, k) * (Dh ** -0.5)
        p = _softmax(scores, axis=-1)
        o = torch.einsum("bhqk,bhkd->bhqd", p, v)
        return (lambda x, *d: x.permute(*d))(o, 0, 2, 1, 3).reshape(B, Sq, Hq * Dh) @ wo

    def _B_ref(x, w_gate, w_up, w_down):
        return (_gelu(x @ w_gate) * (x @ w_up)) @ w_down

    x = A_x
    xn = _layernorm(x, axis=-1)
    return x + _A_ref(xn, A_k_cache, A_v_cache, A_wq, A_wo) + _B_ref(xn, B_w_gate, B_w_up, B_w_down)
```

```cpp
#include <hip/hip_runtime.h>
#include <hip/hip_bf16.h>
#include <stdint.h>

#define MIMIC 0

typedef unsigned u32x4 __attribute__((ext_vector_type(4)));
typedef float f32x2 __attribute__((ext_vector_type(2)));
typedef __bf16 bf16x2 __attribute__((ext_vector_type(2)));

constexpr int NB = 4, DM = 640, NH = 32, DH = 192, SK = 8192, WIN = 2048, FF = 1536;
constexpr int QN = NH * DH;

__device__ __forceinline__ float bf_lo(unsigned u) { return __uint_as_float(u << 16); }
__device__ __forceinline__ float bf_hi(unsigned u) { return __uint_as_float(u & 0xffff0000u); }
__device__ __forceinline__ unsigned cvtpk(float lo, float hi) {
  unsigned r; asm("v_cvt_pk_bf16_f32 %0, %1, %2" : "=v"(r) : "v"(lo), "v"(hi)); return r;
}
__device__ __forceinline__ float rbf(float x) { return bf_lo(cvtpk(x, x)); }
__device__ __forceinline__ float dot2(unsigned k, unsigned q, float acc) {
  return __builtin_amdgcn_fdot2_f32_bf16(__builtin_bit_cast(bf16x2, k), __builtin_bit_cast(bf16x2, q), acc, false);
}
template <int CTRL> __device__ __forceinline__ float dpp(float x) {
  return __builtin_bit_cast(float, __builtin_amdgcn_mov_dpp(__builtin_bit_cast(int, x), CTRL, 0xf, 0xf, true));
}
constexpr int XOR1 = 0xB1, XOR2 = 0x4E, XOR7 = 0x141, XOR8 = 0x128;
__device__ __forceinline__ float x16_sum(float x) {
  auto s = __builtin_amdgcn_permlane16_swap(__float_as_uint(x), __float_as_uint(x), false, false);
  x = __uint_as_float(s[0]) + __uint_as_float(s[1]);
  auto t = __builtin_amdgcn_permlane32_swap(__float_as_uint(x), __float_as_uint(x), false, false);
  return __uint_as_float(t[0]) + __uint_as_float(t[1]);
}
__device__ __forceinline__ float x16_max(float x) {
  auto s = __builtin_amdgcn_permlane16_swap(__float_as_uint(x), __float_as_uint(x), false, false);
  x = fmaxf(__uint_as_float(s[0]), __uint_as_float(s[1]));
  auto t = __builtin_amdgcn_permlane32_swap(__float_as_uint(x), __float_as_uint(x), false, false);
  return fmaxf(__uint_as_float(t[0]), __uint_as_float(t[1]));
}
__device__ __forceinline__ float wave_sum(float x) {
  x += dpp<XOR1>(x); x += dpp<XOR2>(x); x += dpp<XOR7>(x); x += dpp<XOR8>(x); return x16_sum(x);
}

__device__ __forceinline__ void ln_row(const unsigned short* x, float* xs, int lane) {
  float v[10];
#pragma unroll
  for (int i = 0; i < 10; ++i) v[i] = __uint_as_float((unsigned)x[lane + 64 * i] << 16);
  float s = 0.f;
#pragma unroll
  for (int i = 0; i < 10; ++i) s += v[i];
  s = wave_sum(s);
  const float mean = s * (1.f / DM);
  float q = 0.f;
#pragma unroll
  for (int i = 0; i < 10; ++i) { float d = v[i] - mean; q += d * d; }
  q = wave_sum(q);
  const float var = q * (1.f / DM);
  const float mb = rbf(mean), vb = rbf(var);
  const float sd = rbf(sqrtf(rbf(vb + 1e-5f)));
#pragma unroll
  for (int i = 0; i < 10; ++i) xs[lane + 64 * i] = rbf(rbf(v[i] - mb) / sd);
}

__global__ void __launch_bounds__(256) k_prep(const unsigned short* x, const unsigned short* wq, const unsigned short* wg,
                                              const unsigned short* wu, unsigned short* qo, float* ho) {
  __shared__ float xs[NB][DM];
  __shared__ float red[2][4][NB][64];
  const int tid = threadIdx.x, lane = tid & 63, w = tid >> 6, r = lane >> 3, c = lane & 7;
  ln_row(x + w * DM, xs[w], lane);
  __syncthreads();
  const bool isq = blockIdx.x < 96;
  const int nmat = isq ? 1 : 2;
  for (int m = 0; m < nmat; ++m) {
    const unsigned short* W = isq ? wq : (m == 0 ? wg : wu);
    const int N = isq ? QN : FF;
    const int col0 = (isq ? blockIdx.x : blockIdx.x - 96) * 64;
    float acc[NB][8];
#pragma unroll
    for (int b = 0; b < NB; ++b)
#pragma unroll
      for (int k = 0; k < 8; ++k) acc[b][k] = 0.f;
    u32x4 wv[20];
#pragma unroll
    for (int i = 0; i < 20; ++i) wv[i] = *(const u32x4*)(W + (size_t)(w * 160 + i * 8 + r) * N + col0 + c * 8);
#pragma unroll
    for (int i = 0; i < 20; ++i) {
      const int row = w * 160 + i * 8 + r;
#pragma unroll
      for (int b = 0; b < NB; ++b) {
        const float xv = xs[b][row];
#pragma unroll
        for (int k = 0; k < 4; ++k) {
          acc[b][2 * k] = fmaf(xv, bf_lo(wv[i][k]), acc[b][2 * k]);
          acc[b][2 * k + 1] = fmaf(xv, bf_hi(wv[i][k]), acc[b][2 * k + 1]);
        }
      }
    }
#pragma unroll
    for (int b = 0; b < NB; ++b)
#pragma unroll
      for (int k = 0; k < 8; ++k) {
        float t = acc[b][k];
        t += dpp<XOR8>(t); t = x16_sum(t);
        if (r == 0) red[m][w][b][c * 8 + k] = t;
      }
  }
  __syncthreads();
  const int b = tid >> 6, col = tid & 63;
  if (isq) {
    float t = red[0][0][b][col] + red[0][1][b][col] + red[0][2][b][col] + red[0][3][b][col];
    qo[b * QN + blockIdx.x * 64 + col] = (unsigned short)(cvtpk(t, t) & 0xffff);
  } else {
    float g = red[0][0][b][col] + red[0][1][b][col] + red[0][2][b][col] + red[0][3][b][col];
    float u = red[1][0][b][col] + red[1][1][b][col] + red[1][2][b][col] + red[1][3][b][col];
    g = rbf(g); u = rbf(u);
    const float inner = 0.7978845608028654f * (g + 0.044715f * g * g * g);
    const float ge = rbf(0.5f * g * (1.f + tanhf(inner)));
    ho[b * FF + (blockIdx.x - 96) * 64 + col] = rbf(ge * u);
  }
}

constexpr int RING = 8;
__global__ void __launch_bounds__(512, 2) k_attn(const unsigned short* kc, const unsigned short* vc, const unsigned short* q,
                                                 float* part) {
  __shared__ float mrg[8][200];
  const int tid = threadIdx.x, lane = tid & 63, w = __builtin_amdgcn_readfirstlane(tid >> 6), r = lane >> 3, c = lane & 7;
  const int wg = blockIdx.x, bh = wg >> 1, half = wg & 1, b = bh >> 5, h = bh & 31;
  const size_t base = ((size_t)bh * SK + (SK - WIN) + half * 1024) * DH;
  const unsigned short* kp = kc + base + (size_t)(w * 8 + r) * DH + c * 8;
  const unsigned short* vp = vc + base + (size_t)(w * 8 + r) * DH + c * 8;
  u32x4 qv[3];
#pragma unroll
  for (int j = 0; j < 3; ++j) qv[j] = *(const u32x4*)(q + b * QN + h * DH + j * 64 + c * 8);
  u32x4 ring[RING][3];
#define LD(p, g, slot) _Pragma("unroll") for (int j = 0; j < 3; ++j) ring[slot][j] = __builtin_nontemporal_load((const u32x4*)((p) + (size_t)(g) * 64 * DH + j * 64))
#pragma unroll
  for (int i = 0; i < RING; ++i) { LD(kp, i, i); }
  float s[16];
#pragma unroll
  for (int g = 0; g < 16; ++g) {
    float d = 0.f;
#pragma unroll
    for (int j = 0; j < 3; ++j)
#pragma unroll
      for (int k = 0; k < 4; ++k) d = dot2(ring[g % RING][j][k], qv[j][k], d);
    if (g + RING < 16) { LD(kp, g + RING, g % RING); } else { LD(vp, g + RING - 16, g % RING); }
    d += dpp<XOR1>(d); d += dpp<XOR2>(d); d += dpp<XOR7>(d);
    s[g] = d * 0.07216878364870322f;
  }
  float m = s[0];
#pragma unroll
  for (int g = 1; g < 16; ++g) m = fmaxf(m, s[g]);
  m = fmaxf(m, dpp<XOR8>(m)); m = x16_max(m);
  float l = 0.f;
#pragma unroll
  for (int g = 0; g < 16; ++g) { s[g] = __builtin_amdgcn_exp2f((s[g] - m) * 1.4426950408889634f); l += s[g]; }
  l += dpp<XOR8>(l); l = x16_sum(l);
  float acc[24];
#pragma unroll
  for (int k = 0; k < 24; ++k) acc[k] = 0.f;
#pragma unroll
  for (int g = 0; g < 16; ++g) {
    const int slot = (g + 16) % RING;
#pragma unroll
    for (int j = 0; j < 3; ++j)
#pragma unroll
      for (int k = 0; k < 4; ++k) {
        acc[j * 8 + 2 * k] = fmaf(s[g], bf_lo(ring[slot][j][k]), acc[j * 8 + 2 * k]);
        acc[j * 8 + 2 * k + 1] = fmaf(s[g], bf_hi(ring[slot][j][k]), acc[j * 8 + 2 * k + 1]);
      }
    if (g + RING < 16) { LD(vp, g + RING, slot); }
  }
#pragma unroll
  for (int k = 0; k < 24; ++k) { float t = acc[k]; t += dpp<XOR8>(t); acc[k] = x16_sum(t); }
  if (r == 0) {
#pragma unroll
    for (int j = 0; j < 3; ++j)
#pragma unroll
      for (int k = 0; k < 8; ++k) mrg[w][8 + j * 64 + c * 8 + k] = acc[j * 8 + k];
    if (c == 0) { mrg[w][0] = m; mrg[w][1] = l; }
  }
  __syncthreads();
  if (tid < DH) {
    float M = mrg[0][0];
#pragma unroll
    for (int i = 1; i < 8; ++i) M = fmaxf(M, mrg[i][0]);
    float L = 0.f, o = 0.f;
#pragma unroll
    for (int i = 0; i < 8; ++i) {
      const float wt = __builtin_amdgcn_exp2f((mrg[i][0] - M) * 1.4426950408889634f);
      L = fmaf(wt, mrg[i][1], L); o = fmaf(wt, mrg[i][8 + tid], o);
    }
    part[wg * 200 + 8 + tid] = o;
    if (tid == 0) { part[wg * 200] = M; part[wg * 200 + 1] = L; }
  }
}

__global__ void __launch_bounds__(128) k_proj(const float* part, const float* hh, const unsigned short* wo, const unsigned short* wd,
                                              float* pout) {
  __shared__ float a[NB][32];
  const int tid = threadIdx.x, i = blockIdx.x;
  if (tid < 120) {
    const int b = tid / 30, rr = tid % 30, row = i * 30 + rr;
    float v;
    if (row < QN) {
      const int h = row / DH, d = row % DH;
      const float* p0 = part + ((b * NH + h) * 2) * 200; const float* p1 = p0 + 200;
      const float M = fmaxf(p0[0], p1[0]);
      const float w0 = __builtin_amdgcn_exp2f((p0[0] - M) * 1.4426950408889634f), w1 = __builtin_amdgcn_exp2f((p1[0] - M) * 1.4426950408889634f);
      v = rbf((w0 * p0[8 + d] + w1 * p1[8 + d]) / (w0 * p0[1] + w1 * p1[1]));
    } else v = hh[b * FF + row - QN];
    a[b][rr] = v;
  }
  __syncthreads();
  if (tid < 80) {
    float acc[NB][8];
#pragma unroll
    for (int b = 0; b < NB; ++b)
#pragma unroll
      for (int k = 0; k < 8; ++k) acc[b][k] = 0.f;
#pragma unroll
    for (int rr = 0; rr < 30; ++rr) {
      const int row = i * 30 + rr;
      const unsigned short* W = row < QN ? wo + (size_t)row * DM : wd + (size_t)(row - QN) * DM;
      const u32x4 wv = *(const u32x4*)(W + tid * 8);
#pragma unroll
      for (int b = 0; b < NB; ++b)
#pragma unroll
        for (int k = 0; k < 4; ++k) {
          acc[b][2 * k] = fmaf(a[b][rr], bf_lo(wv[k]), acc[b][2 * k]);
          acc[b][2 * k + 1] = fmaf(a[b][rr], bf_hi(wv[k]), acc[b][2 * k + 1]);
        }
    }
#pragma unroll
    for (int b = 0; b < NB; ++b)
#pragma unroll
      for (int k = 0; k < 8; ++k) pout[(size_t)i * 2560 + b * DM + tid * 8 + k] = acc[b][k];
  }
}

__global__ void __launch_bounds__(256) k_fin(const float* pout, const unsigned short* x, unsigned short* out) {
  __shared__ float red[4][64];
  const int tid = threadIdx.x, o = blockIdx.x * 64 + (tid & 63), w = tid >> 6;
  float s = 0.f;
#pragma unroll 16
  for (int i = 0; i < 64; ++i) s += pout[(size_t)(w * 64 + i) * 2560 + o];
  red[w][tid & 63] = s;
  __syncthreads();
  if (tid < 64) {
    const float t = red[0][tid] + red[1][tid] + red[2][tid] + red[3][tid] + __uint_as_float((unsigned)x[o] << 16);
    out[o] = (unsigned short)(cvtpk(t, t) & 0xffff);
  }
}

extern "C" void kernel_launch(void* const* d_in, const int* in_sizes, int n_in, void* d_out, int out_size, void* d_ws,
                              size_t ws_size, hipStream_t stream) {
  const unsigned short* x = (const unsigned short*)d_in[0];
  const unsigned short* kc = (const unsigned short*)d_in[1];
  const unsigned short* vc = (const unsigned short*)d_in[2];
  const unsigned short* wq = (const unsigned short*)d_in[3];
  const unsigned short* wo = (const unsigned short*)d_in[4];
  const unsigned short* wg = (const unsigned short*)d_in[6];
  const unsigned short* wu = (const unsigned short*)d_in[7];
  const unsigned short* wd = (const unsigned short*)d_in[8];
  char* ws = (char*)d_ws;
  unsigned short* q = (unsigned short*)ws;
  float* hh = (float*)(ws + 65536);
  float* part = (float*)(ws + 131072);
  float* pout = (float*)(ws + 524288);
  k_prep<<<120, 256, 0, stream>>>(x, wq, wg, wu, q, hh);
  k_attn<<<256, 512, 0, stream>>>(kc, vc, q, part);
  k_proj<<<256, 128, 0, stream>>>(part, hh, wo, wd, pout);
  k_fin<<<40, 256, 0, stream>>>(pout, x, (unsigned short*)d_out);
}
```

```cpp
#include <hip/hip_runtime.h>
#include <hip/hip_bf16.h>
#include <stdint.h>

#define RING 10
#define W0 2
#define LEAD 2
#define WQW 3
#define ROTM 1
#define STAMP 0

typedef unsigned short u16;
typedef unsigned u32x2 __attribute__((ext_vector_type(2)));
typedef unsigned u32x4 __attribute__((ext_vector_type(4)));
typedef float f32x2 __attribute__((ext_vector_type(2)));
typedef float f32x4 __attribute__((ext_vector_type(4)));
typedef short s16x4 __attribute__((ext_vector_type(4)));
typedef short s16x8 __attribute__((ext_vector_type(8)));
typedef __bf16 bf16x2 __attribute__((ext_vector_type(2)));
typedef __bf16 bf16x8 __attribute__((ext_vector_type(8)));
#define LAS __attribute__((address_space(3)))
#define GAS __attribute__((address_space(1)))
typedef __amdgpu_buffer_rsrc_t rsrc_t;

constexpr int DM = 640, NH = 32, DH = 192, SK = 8192, WIN = 2048, FF = 1536, QN = NH * DH;
constexpr int NTHR = 576;
constexpr float LOG2E = 1.4426950408889634f;
constexpr unsigned SPIN_CAP = 1u << 18;

constexpr unsigned WS_QP = 4096;
constexpr unsigned WS_GU = WS_QP + 32 * 8 * 768 * 8;
constexpr unsigned WS_OP = WS_GU + 32 * 8 * 192 * 8;
constexpr unsigned WS_YP = WS_OP + 32 * 8 * 208 * 8;
constexpr unsigned WS_END = WS_YP + 8 * 20 * 32 * 16 * 8;

constexpr int RS_WQ = 416, RS_GU = 96, RS_WO = 160, RS_O = 496;
constexpr int L_WQ = 0;
constexpr int L_GU = L_WQ + 80 * RS_WQ;
constexpr int L_WO = L_GU + 160 * RS_GU;
constexpr int L_MRG = L_WO + 240 * RS_WO + 512;
constexpr int L_Q = L_MRG + 8 * 800;
constexpr int L_O = L_Q + 384;
constexpr int RS_X = 1296;
constexpr int L_X = L_O + 4 * RS_O;
constexpr int L_END = L_X + 4 * RS_X;

__device__ __forceinline__ float bf_lo(unsigned u) { return __uint_as_float(u << 16); }
__device__ __forceinline__ float bf_hi(unsigned u) { return __uint_as_float(u & 0xffff0000u); }
__device__ __forceinline__ unsigned cvtpk(float lo, float hi) {
  bf16x2 r = {(__bf16)lo, (__bf16)hi};
  return __builtin_bit_cast(unsigned, r);
}
__device__ __forceinline__ float rbf(float x) { return bf_lo(cvtpk(x, 0.f)); }
__device__ __forceinline__ float dot2(unsigned k, unsigned q, float acc) {
  return __builtin_amdgcn_fdot2_f32_bf16(__builtin_bit_cast(bf16x2, k), __builtin_bit_cast(bf16x2, q), acc, false);
}
template <int CTRL> __device__ __forceinline__ float dpp(float x) {
  return __builtin_bit_cast(float, __builtin_amdgcn_mov_dpp(__builtin_bit_cast(int, x), CTRL, 0xf, 0xf, true));
}
constexpr int XOR1 = 0xB1, XOR2 = 0x4E, XOR7 = 0x141, XOR8 = 0x128;
__device__ __forceinline__ float swap16_add(float a, float b) {
  auto s = __builtin_amdgcn_permlane16_swap(__float_as_uint(a), __float_as_uint(b), false, false);
  return __uint_as_float(s[0]) + __uint_as_float(s[1]);
}
__device__ __forceinline__ float swap32_add(float a, float b) {
  auto s = __builtin_amdgcn_permlane32_swap(__float_as_uint(a), __float_as_uint(b), false, false);
  return __uint_as_float(s[0]) + __uint_as_float(s[1]);
}
__device__ __forceinline__ float x16_sum(float x) { x = swap16_add(x, x); return swap32_add(x, x); }
__device__ __forceinline__ float x16_max(float x) {
  auto s = __builtin_amdgcn_permlane16_swap(__float_as_uint(x), __float_as_uint(x), false, false);
  x = fmaxf(__uint_as_float(s[0]), __uint_as_float(s[1]));
  auto t = __builtin_amdgcn_permlane32_swap(__float_as_uint(x), __float_as_uint(x), false, false);
  return fmaxf(__uint_as_float(t[0]), __uint_as_float(t[1]));
}
__device__ __forceinline__ void bar() { asm volatile("s_waitcnt lgkmcnt(0)\n\ts_barrier" ::: "memory"); }
template <int N> __device__ __forceinline__ void wait_vm() { asm volatile("s_waitcnt vmcnt(%0)" ::"n"(N) : "memory"); }
__device__ __forceinline__ rsrc_t mk_rsrc(const void* p, unsigned bytes) {
  return __builtin_amdgcn_make_buffer_rsrc((void*)p, 0, bytes, 0x00020000);
}
constexpr int AUX_NT = 2, AUX_SC1 = 16;
__device__ __forceinline__ void put_granule(unsigned char* ws, unsigned off, float v, unsigned tag) {
  __hip_atomic_store((GAS unsigned long long*)(ws + off), ((unsigned long long)tag << 32) | __float_as_uint(v), __ATOMIC_RELAXED,
                     __HIP_MEMORY_SCOPE_AGENT);
}
__device__ __forceinline__ u32x4 get_granules2(rsrc_t rs, unsigned off) {
  return __builtin_amdgcn_raw_buffer_load_b128(rs, off, 0, AUX_SC1);
}
__device__ __forceinline__ s16x4 tr_read(unsigned char* lds, int off) {
  return __builtin_amdgcn_ds_read_tr16_b64_v4i16((LAS s16x4*)(lds + off));
}
__device__ __forceinline__ f32x4 mfma32(s16x4 a0, s16x4 a1, s16x4 b0, s16x4 b1, f32x4 c) {
  const s16x8 a = __builtin_shufflevector(a0, a1, 0, 1, 2, 3, 4, 5, 6, 7), b = __builtin_shufflevector(b0, b1, 0, 1, 2, 3, 4, 5, 6, 7);
  return __builtin_amdgcn_mfma_f32_16x16x32_bf16(__builtin_bit_cast(bf16x8, a), __builtin_bit_cast(bf16x8, b), c, 0, 0, 0);
}

__device__ __forceinline__ void ln_stats(const u32x4 (&xs)[5], float& mb, float& rs) {
  float s = 0.f;
#pragma unroll
  for (int i = 0; i < 5; ++i)
#pragma unroll
    for (int k = 0; k < 4; ++k) s += bf_lo(xs[i][k]) + bf_hi(xs[i][k]);
  s += dpp<XOR1>(s); s += dpp<XOR2>(s); s = x16_sum(s);
  const float mean = s * (1.f / DM);
  float q = 0.f;
#pragma unroll
  for (int i = 0; i < 5; ++i)
#pragma unroll
    for (int k = 0; k < 4; ++k) {
      const float a = bf_lo(xs[i][k]) - mean, c = bf_hi(xs[i][k]) - mean;
      q = fmaf(a, a, q); q = fmaf(c, c, q);
    }
  q += dpp<XOR1>(q); q += dpp<XOR2>(q); q = x16_sum(q);
  mb = rbf(mean);
  rs = __builtin_amdgcn_rcpf(rbf(sqrtf(rbf(rbf(q * (1.f / DM)) + 1e-5f))));
}
__device__ __forceinline__ u32x4 ln_apply(u32x4 v, float mb, float rs) {
  u32x4 r;
#pragma unroll
  for (int k = 0; k < 4; ++k) r[k] = cvtpk(rbf(bf_lo(v[k]) - mb) * rs, rbf(bf_hi(v[k]) - mb) * rs);
  return r;
}
__device__ __forceinline__ void dma16(rsrc_t rs, unsigned char* lds, int ldsoff, int voff) {
  __builtin_amdgcn_raw_ptr_buffer_load_lds(rs, (LAS void*)(lds + ldsoff), 16, voff, 0, 0, AUX_NT);
}

#define ST(i) do {} while (0)
#define ST_END() do {} while (0)

struct Params {
  const u16 *x, *kc, *vc, *wq, *wo, *wg, *wu, *wd;
  u16* out;
  unsigned char* ws;
};

__global__ void __launch_bounds__(NTHR, 3) fused_decode(Params p) {
  __shared__ __attribute__((aligned(16))) unsigned char lds[L_END];
  const int tid = threadIdx.x, lane = tid & 63, w = __builtin_amdgcn_readfirstlane(tid >> 6);
  const int wg = blockIdx.x, idx = wg >> 3, h = (wg & 7) * 4 + (idx >> 3), j = idx & 7;
  const int g4 = lane >> 4, i16 = lane & 15, bl = (lane >> 2) & 3;
  const int tr_row = 4 * g4 + bl, tr_col = 8 * (lane & 3);
  const rsrc_t rws = mk_rsrc(p.ws, WS_END);
  const unsigned epoch_v = __hip_atomic_load((GAS unsigned*)p.ws, __ATOMIC_RELAXED, __HIP_MEMORY_SCOPE_AGENT);
  unsigned tag;
  unsigned stampv = 0;

  if (w > 0) {
    ST(0);
    const int sw = w - 1, r = lane >> 3, c = lane & 7;
    u32x4 wv[4];
    int wl[4];
    {
      const rsrc_t rq = mk_rsrc(p.wq + (size_t)(80 * j + 10 * sw) * QN + h * DH, 10 * QN * 2);
#pragma unroll
      for (int i = 0; i < 4; ++i) {
        const int ci = min(i * 64 + lane, 239), row = ci / 24, cc = ci - row * 24;
        wv[i] = __builtin_amdgcn_raw_buffer_load_b128(rq, row * (QN * 2) + cc * 16, 0, AUX_NT);
        wl[i] = L_WQ + (10 * sw + row) * RS_WQ + cc * 16;
      }
    }
    const size_t kvoff = ((size_t)((j >> 1) * NH + h) * SK + (SK - WIN) + (j & 1) * 1024) * DH;
    const rsrc_t rk = mk_rsrc(p.kc + kvoff, 1024 * DH * 2), rv = mk_rsrc(p.vc + kvoff, 1024 * DH * 2);
    const int voff = r * 384 + c * 16;
    const int rot = ROTM ? idx : 0;
    u32x4 ring[RING][3];
#define ISSUE(G)                                                                                              \
  do {                                                                                                        \
    const int so_ = (((((G) & 15) + rot) & 15) * 8 + sw) * 3072;                                              \
    _Pragma("unroll") for (int jj = 0; jj < 3; ++jj) ring[(G) % RING][jj] =                                   \
        __builtin_amdgcn_raw_buffer_load_b128((G) < 16 ? rk : rv, voff + jj * 128, so_, AUX_NT);              \
  } while (0)
    __builtin_amdgcn_sched_barrier(0);
    wait_vm<WQW>();
#pragma unroll
    for (int G = 0; G < W0; ++G) ISSUE(G);
    __builtin_amdgcn_sched_barrier(0);
    tag = __builtin_amdgcn_readfirstlane(epoch_v) + 1u;
#pragma unroll
    for (int i = 0; i < 4; ++i) *(LAS u32x4*)(lds + wl[i]) = wv[i];
    ST(1);
    bar();
    if (sw < 6) {
      s16x4 af[5];
#pragma unroll
      for (int s = 0; s < 5; ++s) af[s] = *(LAS s16x4*)(lds + L_X + bl * RS_X + (80 * j + 16 * s + 4 * g4) * 2);
#pragma unroll
      for (int t = 0; t < 2; ++t) {
        const int n0 = 32 * sw + 16 * t;
        s16x4 bf[5];
#pragma unroll
        for (int s = 0; s < 5; ++s) bf[s] = tr_read(lds, L_WQ + (16 * s + tr_row) * RS_WQ + n0 * 2 + tr_col);
        const f32x4 z = {0.f, 0.f, 0.f, 0.f};
        f32x4 d = mfma32(af[0], af[1], bf[0], bf[1], z);
        d = mfma32(af[2], af[3], bf[2], bf[3], d);
        d = mfma32(af[4], s16x4{0, 0, 0, 0}, bf[4], bf[4], d);
        put_granule(p.ws, WS_QP + (((h * 8 + j) * 4 + g4) * DH + n0 + i16) * 8, d[0], tag);
      }
    }
    ST(2);
    __builtin_amdgcn_sched_barrier(0);
#pragma unroll
    for (int G = W0; G < RING; ++G) {
      wait_vm<3 * (W0 - 1)>();
      ISSUE(G);
      __builtin_amdgcn_sched_barrier(0);
    }
    ST(3);
    bar();
    ST(4);
    u32x4 qv[3];
#pragma unroll
    for (int jj = 0; jj < 3; ++jj) qv[jj] = *(LAS u32x4*)(lds + L_Q + jj * 128 + c * 16);
    float sc[16];
#pragma unroll
    for (int G = 0; G < 16; ++G) {
      float d0 = 0.f, d1 = 0.f, d2 = 0.f;
#pragma unroll
      for (int k = 0; k < 4; ++k) {
        d0 = dot2(ring[G % RING][0][k], qv[0][k], d0);
        d1 = dot2(ring[G % RING][1][k], qv[1][k], d1);
        d2 = dot2(ring[G % RING][2][k], qv[2][k], d2);
      }
      float d = (d0 + d1) + d2;
      __builtin_amdgcn_sched_barrier(0);
      if (G + LEAD >= RING) ISSUE(G + LEAD);
      __builtin_amdgcn_sched_barrier(0);
      d += dpp<XOR1>(d); d += dpp<XOR2>(d); d += dpp<XOR7>(d);
      sc[G] = rbf(rbf(d) * 0.07216878364870322f);
    }
    ST(5);
    float m = sc[0];
#pragma unroll
    for (int G = 1; G < 16; ++G) m = fmaxf(m, sc[G]);
    m = fmaxf(m, dpp<XOR8>(m));
    m = x16_max(m);
    float l = 0.f;
#pragma unroll
    for (int G = 0; G < 16; ++G) { sc[G] = __builtin_amdgcn_exp2f((sc[G] - m) * LOG2E); l += sc[G]; }
    l += dpp<XOR8>(l);
    l = x16_sum(l);
    f32x2 acc[12];
#pragma unroll
    for (int k = 0; k < 12; ++k) acc[k] = f32x2{0.f, 0.f};
#pragma unroll
    for (int G = 16; G < 32; ++G) {
      const f32x2 pp = {sc[G - 16], sc[G - 16]};
#pragma unroll
      for (int jj = 0; jj < 3; ++jj)
#pragma unroll
        for (int k = 0; k < 4; ++k) {
          const unsigned u = ring[G % RING][jj][k];
          acc[jj * 4 + k] = __builtin_elementwise_fma(f32x2{bf_lo(u), bf_hi(u)}, pp, acc[jj * 4 + k]);
        }
      __builtin_amdgcn_sched_barrier(0);
      if (G + LEAD < 32) ISSUE(G + LEAD);
      __builtin_amdgcn_sched_barrier(0);
    }
#undef ISSUE
    ST(6);
    ST(17);
    float t12[12], t6[6];
#pragma unroll
    for (int k = 0; k < 12; ++k) t12[k] = swap32_add(acc[k >> 1][k & 1], acc[(k + 12) >> 1][k & 1]);
#pragma unroll
    for (int k = 0; k < 6; ++k) t6[k] = swap16_add(t12[k], t12[k + 6]);
    float* mrg = (float*)(lds + L_MRG) + sw * 200;
#pragma unroll
    for (int k = 0; k < 3; ++k) {
      const float a = t6[k] + dpp<XOR8>(t6[k]), b2 = t6[k + 3] + dpp<XOR8>(t6[k + 3]);
      const int e = 3 * r + k;
      mrg[(e >> 3) * 64 + c * 8 + (e & 7)] = (lane & 8) ? b2 : a;
    }
    if (lane == 0) { mrg[192] = m; mrg[193] = l; }
  } else {
    __builtin_amdgcn_s_setprio(3);
    const int jk = j & 3;
    {
      u32x4 xs[5];
#pragma unroll
      for (int s = 0; s < 5; ++s) xs[s] = *(const u32x4*)(p.x + bl * DM + (g4 * 4 + (lane & 3) + 16 * s) * 8);
      tag = __builtin_amdgcn_readfirstlane(epoch_v) + 1u;
      float mb, rs;
      ln_stats(xs, mb, rs);
#pragma unroll
      for (int s = 0; s < 5; ++s) *(LAS u32x4*)(lds + L_X + bl * RS_X + (g4 * 4 + (lane & 3) + 16 * s) * 16) = ln_apply(xs[s], mb, rs);
    }
    ST(7);
    bar();
    {
      const unsigned qb = WS_QP + ((h * 8 * 4 + (j >> 1)) * DH) * 8;
      const unsigned o0 = qb + lane * 16, o1 = qb + (lane >> 5) * 4 * (768 * 8) + (64 + (lane & 31)) * 16;
      u32x4 a[8], b2[4];
      for (unsigned spins = 0; spins < SPIN_CAP; ++spins) {
        asm volatile("" ::: "memory");
#pragma unroll
        for (int i = 0; i < 8; ++i) a[i] = get_granules2(rws, o0 + i * (768 * 8));
#pragma unroll
        for (int i = 0; i < 4; ++i) b2[i] = get_granules2(rws, o1 + i * (768 * 8));
        bool ok = true;
#pragma unroll
        for (int i = 0; i < 8; ++i) ok &= (a[i][1] == tag) & (a[i][3] == tag);
#pragma unroll
        for (int i = 0; i < 4; ++i) ok &= (b2[i][1] == tag) & (b2[i][3] == tag);
        if (__all(ok)) break;
      }
      float s0 = 0.f, s1 = 0.f, u0 = 0.f, u1 = 0.f;
#pragma unroll
      for (int i = 0; i < 8; ++i) { s0 += __uint_as_float(a[i][0]); s1 += __uint_as_float(a[i][2]); }
#pragma unroll
      for (int i = 0; i < 4; ++i) { u0 += __uint_as_float(b2[i][0]); u1 += __uint_as_float(b2[i][2]); }
      u0 = swap32_add(u0, u0); u1 = swap32_add(u1, u1);
      *(LAS unsigned*)(lds + L_Q + lane * 4) = cvtpk(s0, s1);
      if (lane < 32) *(LAS unsigned*)(lds + L_Q + 256 + lane * 4) = cvtpk(u0, u1);
    }
    ST(8);
    bar();
    {
      const rsrc_t rg = mk_rsrc((j < 4 ? p.wg : p.wu) + (size_t)160 * jk * FF + 48 * h, 160 * FF * 2);
#pragma unroll
      for (int i = 0; i < 15; ++i) {
        const int ci = i * 64 + lane, row = ci / 6, cc = ci - row * 6;
        dma16(rg, lds, L_GU + i * 1024, row * (FF * 2) + cc * 16);
      }
      s16x4 af[10];
#pragma unroll
      for (int s = 0; s < 10; ++s) af[s] = *(LAS s16x4*)(lds + L_X + bl * RS_X + (160 * jk + 16 * s + 4 * g4) * 2);
      wait_vm<0>();
#pragma unroll
      for (int t = 0; t < 3; ++t) {
        f32x4 d = {0.f, 0.f, 0.f, 0.f};
#pragma unroll
        for (int s = 0; s < 10; s += 2) {
          const s16x4 b0 = tr_read(lds, L_GU + (16 * s + tr_row) * RS_GU + 32 * t + tr_col);
          const s16x4 b1 = tr_read(lds, L_GU + (16 * s + 16 + tr_row) * RS_GU + 32 * t + tr_col);
          d = mfma32(af[s], af[s + 1], b0, b1, d);
        }
        put_granule(p.ws, WS_GU + (((h * 8 + j) * 4 + g4) * 48 + 16 * t + i16) * 8, d[0], tag);
      }
    }
    ST(9);
    __builtin_amdgcn_sched_barrier(0);
    {
      const rsrc_t ro = mk_rsrc(p.wo + (size_t)DH * h * DM + 80 * j, DH * DM * 2 - 160 * j);
      const rsrc_t rd = mk_rsrc(p.wd + (size_t)48 * h * DM + 80 * j, 48 * DM * 2 - 160 * j);
#pragma unroll
      for (int i = 0; i < 30; ++i) {
        const int ci = i * 64 + lane, row = ci / 10, cc = ci - row * 10;
        dma16(ro, lds, L_WO + i * 1024, row * (DM * 2) + cc * 16);
        if (i % 8 == 7) wait_vm<4>();
      }
#pragma unroll
      for (int i = 0; i < 8; ++i) {
        const int ci = min(i * 64 + lane, 479), row = ci / 10, cc = ci - row * 10;
        dma16(rd, lds, L_WO + 30720 + i * 1024, row * (DM * 2) + cc * 16);
      }
      wait_vm<0>();
    }
    ST(10);
    __builtin_amdgcn_sched_barrier(0);
    {
#pragma unroll
      for (int pass = 0; pass < 2; ++pass) {
        const int it = pass == 0 ? lane : 64 + (lane & 31), bb = it / 24, pr = it - bb * 24;
        const unsigned o0 = WS_GU + ((h * 8 * 4 + bb) * 48 + 2 * pr) * 8;
        u32x4 a[8];
        for (unsigned spins = 0; spins < SPIN_CAP; ++spins) {
          asm volatile("" ::: "memory");
#pragma unroll
          for (int i = 0; i < 8; ++i) a[i] = get_granules2(rws, o0 + i * (192 * 8));
          bool ok = true;
#pragma unroll
          for (int i = 0; i < 8; ++i) ok &= (a[i][1] == tag) & (a[i][3] == tag);
          if (__all(ok)) break;
          __builtin_amdgcn_s_sleep(8);
        }
        float hv[2];
#pragma unroll
        for (int e = 0; e < 2; ++e) {
          const float gg = rbf((__uint_as_float(a[0][2 * e]) + __uint_as_float(a[1][2 * e])) + (__uint_as_float(a[2][2 * e]) + __uint_as_float(a[3][2 * e])));
          const float uu = rbf((__uint_as_float(a[4][2 * e]) + __uint_as_float(a[5][2 * e])) + (__uint_as_float(a[6][2 * e]) + __uint_as_float(a[7][2 * e])));
          const float inner = 0.7978845608028654f * (gg + 0.044715f * gg * gg * gg);
          hv[e] = rbf(0.5f * gg * (1.f + tanhf(inner))) * uu;
        }
        *(LAS unsigned*)(lds + L_O + bb * RS_O + (DH + 2 * pr) * 2) = cvtpk(hv[0], hv[1]);
      }
    }
    ST(11);
  }

  bar();
  ST(12);
  const float* mrg = (const float*)(lds + L_MRG);
  if (w < 4) {
    float M = mrg[192];
#pragma unroll
    for (int i = 1; i < 8; ++i) M = fmaxf(M, mrg[i * 200 + 192]);
    const int d = w < 3 ? tid : 193;
    float o = 0.f;
#pragma unroll
    for (int i = 0; i < 8; ++i) o = fmaf(__builtin_amdgcn_exp2f((mrg[i * 200 + 192] - M) * LOG2E), mrg[i * 200 + d], o);
    if (w < 3) put_granule(p.ws, WS_OP + ((h * 8 + j) * 208 + tid) * 8, o, tag);
    else if (lane < 2) put_granule(p.ws, WS_OP + ((h * 8 + j) * 208 + 192 + lane) * 8, lane == 0 ? M : o, tag);
  }
  s16x4 wf[15];
  if (w < 5) {
#pragma unroll
    for (int s = 0; s < 15; ++s) wf[s] = tr_read(lds, L_WO + (16 * s + tr_row) * RS_WO + 32 * w + tr_col);
  }
  ST(13);
  if (w < 6) {
    const int bb = tid / 96, pr = tid - bb * 96;
    const unsigned ob = WS_OP + (h * 8 + 2 * bb) * 208 * 8;
    u32x4 a0, a1, m0, m1;
    for (unsigned spins = 0; spins < SPIN_CAP; ++spins) {
      asm volatile("" ::: "memory");
      a0 = get_granules2(rws, ob + pr * 16);
      a1 = get_granules2(rws, ob + 208 * 8 + pr * 16);
      m0 = get_granules2(rws, ob + 192 * 8);
      m1 = get_granules2(rws, ob + 208 * 8 + 192 * 8);
      const bool ok = (a0[1] == tag) & (a0[3] == tag) & (a1[1] == tag) & (a1[3] == tag) & (m0[1] == tag) & (m0[3] == tag) & (m1[1] == tag) & (m1[3] == tag);
      if (__all(ok)) break;
    }
    const float ma = __uint_as_float(m0[0]), mb2 = __uint_as_float(m1[0]), M = fmaxf(ma, mb2);
    const float wa = __builtin_amdgcn_exp2f((ma - M) * LOG2E), wb = __builtin_amdgcn_exp2f((mb2 - M) * LOG2E);
    const float inv = __builtin_amdgcn_rcpf(fmaf(wa, __uint_as_float(m0[2]), wb * __uint_as_float(m1[2])));
    const float ua = wa * inv, ub = wb * inv;
    *(LAS unsigned*)(lds + L_O + bb * RS_O + pr * 4) =
        cvtpk(fmaf(ua, __uint_as_float(a0[0]), ub * __uint_as_float(a1[0])), fmaf(ua, __uint_as_float(a0[2]), ub * __uint_as_float(a1[2])));
  }
  ST(14);
  bar();
  if (w < 5) {
    s16x4 of[15];
#pragma unroll
    for (int s = 0; s < 15; ++s) of[s] = *(LAS s16x4*)(lds + L_O + bl * RS_O + (16 * s + 4 * g4) * 2);
    f32x4 d0 = {0.f, 0.f, 0.f, 0.f}, d1 = d0;
#pragma unroll
    for (int s = 0; s < 8; s += 2) d0 = mfma32(of[s], of[s + 1], wf[s], wf[s + 1], d0);
#pragma unroll
    for (int s = 8; s < 14; s += 2) d1 = mfma32(of[s], of[s + 1], wf[s], wf[s + 1], d1);
    d1 = mfma32(of[14], s16x4{0, 0, 0, 0}, wf[14], wf[14], d1);
    put_granule(p.ws, WS_YP + (((j * 20 + 4 * w + g4) * 32 + h) * 16 + i16) * 8, d0[0] + d1[0], tag);
    ST(15);
  } else if (w == 6 && h < 20) {
    const int oi = (h & 3) * DM + 80 * j + 16 * (h >> 2) + 2 * (lane & 7);
    const unsigned xr = *(const unsigned*)(p.x + oi);
    const unsigned yb = WS_YP + (j * 20 + h) * 512 * 8 + lane * 16;
    u32x4 a[4];
    for (unsigned spins = 0; spins < SPIN_CAP; ++spins) {
      asm volatile("" ::: "memory");
#pragma unroll
      for (int i = 0; i < 4; ++i) a[i] = get_granules2(rws, yb + i * 1024);
      bool ok = true;
#pragma unroll
      for (int i = 0; i < 4; ++i) ok &= (a[i][1] == tag) & (a[i][3] == tag);
      if (__all(ok)) break;
    }
    float s0 = (__uint_as_float(a[0][0]) + __uint_as_float(a[1][0])) + (__uint_as_float(a[2][0]) + __uint_as_float(a[3][0]));
    float s1 = (__uint_as_float(a[0][2]) + __uint_as_float(a[1][2])) + (__uint_as_float(a[2][2]) + __uint_as_float(a[3][2]));
    s0 += dpp<XOR8>(s0); s1 += dpp<XOR8>(s1);
    s0 = x16_sum(s0); s1 = x16_sum(s1);
    if (lane < 8) *(unsigned*)(p.out + oi) = cvtpk(bf_lo(xr) + s0, bf_hi(xr) + s1);
    if (wg == 0 && lane == 0) __hip_atomic_store((GAS unsigned*)p.ws, tag, __ATOMIC_RELAXED, __HIP_MEMORY_SCOPE_AGENT);
    ST(16);
  }
  ST_END();
}

extern "C" void kernel_launch(void* const* d_in, const int* in_sizes, int n_in, void* d_out, int out_size, void* d_ws,
                              size_t ws_size, hipStream_t stream) {
  Params p;
  p.x = (const u16*)d_in[0];
  p.kc = (const u16*)d_in[1];
  p.vc = (const u16*)d_in[2];
  p.wq = (const u16*)d_in[3];
  p.wo = (const u16*)d_in[4];
  p.wg = (const u16*)d_in[6];
  p.wu = (const u16*)d_in[7];
  p.wd = (const u16*)d_in[8];
  p.out = (u16*)d_out;
  p.ws = (unsigned char*)d_ws;
  fused_decode<<<256, NTHR, 0, stream>>>(p);
}
```

```cpp
#include <hip/hip_runtime.h>
#include <hip/hip_bf16.h>
#include <stdint.h>

#define RING 8
#define W0 2
#define LEAD 2
#define CATCH 4
#define WQW 4
#define ROTM 1
#define CODELEN 21504
#define XP 0
#define SL1 4
#define SL2 4
#define CODEPF 0
#define PAIRS 1
#define QMASK 1
#define SLQ 8
#define SLQ0 8
#define GLD 16
#define GST 16
#define TOUCH 0
#define FLY 4
#define WSPOS 0
#define STAMP 0

typedef unsigned short u16;
typedef unsigned u32x2 __attribute__((ext_vector_type(2)));
typedef unsigned u32x4 __attribute__((ext_vector_type(4)));
typedef float f32x2 __attribute__((ext_vector_type(2)));
typedef float f32x4 __attribute__((ext_vector_type(4)));
typedef short s16x4 __attribute__((ext_vector_type(4)));
typedef short s16x8 __attribute__((ext_vector_type(8)));
typedef __bf16 bf16x2 __attribute__((ext_vector_type(2)));
typedef __bf16 bf16x8 __attribute__((ext_vector_type(8)));
#define LAS __attribute__((address_space(3)))
#define GAS __attribute__((address_space(1)))
typedef __amdgpu_buffer_rsrc_t rsrc_t;

constexpr int DM = 640, NH = 32, DH = 192, SK = 8192, WIN = 2048, FF = 1536, QN = NH * DH;
constexpr int NTHR = 768;
constexpr float LOG2E = 1.4426950408889634f;
constexpr unsigned SPIN_CAP = 1u << 18;

constexpr unsigned WS_QP = 4096;
constexpr unsigned WS_GU = WS_QP + 32 * 8 * 768 * 8;
constexpr unsigned WS_OP = WS_GU + 32 * 8 * 192 * 8;
constexpr unsigned WS_YP = WS_OP + 32 * 8 * 208 * 8;
constexpr unsigned WS_END = WS_YP + 8 * 20 * 32 * 16 * 8;

constexpr int RS_WQ = 384, RS_GU = 96, RS_WO = 160, RS_O = 496;
constexpr int L_WQ = 0;
constexpr int L_GU = L_WQ + 80 * RS_WQ;
constexpr int L_WO = L_GU + 160 * RS_GU;
constexpr int L_MRG = L_WO + 240 * RS_WO + 512;
constexpr int L_Q = L_MRG + 8 * 800;
constexpr int L_O = L_Q + 384;
constexpr int RS_X = 1296;
constexpr int L_X = L_O + 4 * RS_O;
constexpr int L_FLAG = L_X + 4 * RS_X;
constexpr int L_END = L_FLAG + 128;

__device__ __forceinline__ float bf_lo(unsigned u) { return __uint_as_float(u << 16); }
__device__ __forceinline__ float bf_hi(unsigned u) { return __uint_as_float(u & 0xffff0000u); }
__device__ __forceinline__ unsigned cvtpk(float lo, float hi) {
  bf16x2 r = {(__bf16)lo, (__bf16)hi};
  return __builtin_bit_cast(unsigned, r);
}
__device__ __forceinline__ float rbf(float x) { return bf_lo(cvtpk(x, 0.f)); }
__device__ __forceinline__ float dot2(unsigned k, unsigned q, float acc) {
  return __builtin_amdgcn_fdot2_f32_bf16(__builtin_bit_cast(bf16x2, k), __builtin_bit_cast(bf16x2, q), acc, false);
}
template <int CTRL> __device__ __forceinline__ float dpp(float x) {
  return __builtin_bit_cast(float, __builtin_amdgcn_mov_dpp(__builtin_bit_cast(int, x), CTRL, 0xf, 0xf, true));
}
constexpr int XOR1 = 0xB1, XOR2 = 0x4E, XOR7 = 0x141, XOR8 = 0x128;
__device__ __forceinline__ float swap16_add(float a, float b) {
  auto s = __builtin_amdgcn_permlane16_swap(__float_as_uint(a), __float_as_uint(b), false, false);
  return __uint_as_float(s[0]) + __uint_as_float(s[1]);
}
__device__ __forceinline__ float swap32_add(float a, float b) {
  auto s = __builtin_amdgcn_permlane32_swap(__float_as_uint(a), __float_as_uint(b), false, false);
  return __uint_as_float(s[0]) + __uint_as_float(s[1]);
}
__device__ __forceinline__ float x16_sum(float x) { x = swap16_add(x, x); return swap32_add(x, x); }
__device__ __forceinline__ float x16_max(float x) {
  auto s = __builtin_amdgcn_permlane16_swap(__float_as_uint(x), __float_as_uint(x), false, false);
  x = fmaxf(__uint_as_float(s[0]), __uint_as_float(s[1]));
  auto t = __builtin_amdgcn_permlane32_swap(__float_as_uint(x), __float_as_uint(x), false, false);
  return fmaxf(__uint_as_float(t[0]), __uint_as_float(t[1]));
}
__device__ __forceinline__ void bar() { asm volatile("s_waitcnt lgkmcnt(0)\n\ts_barrier" ::: "memory"); }
template <int N> __device__ __forceinline__ void wait_vm() { asm volatile("s_waitcnt vmcnt(%0)" ::"n"(N) : "memory"); }
__device__ __forceinline__ rsrc_t mk_rsrc(const void* p, unsigned bytes) {
  return __builtin_amdgcn_make_buffer_rsrc((void*)p, 0, bytes, 0x00020000);
}
constexpr int AUX_NT = 2, AUX_SC1 = 16;
__device__ __forceinline__ void put_granule(unsigned char* ws, unsigned off, float v, unsigned tag) {
  __hip_atomic_store((GAS unsigned long long*)(ws + off), ((unsigned long long)tag << 32) | __float_as_uint(v), __ATOMIC_RELAXED,
                     __HIP_MEMORY_SCOPE_AGENT);
}
__device__ __forceinline__ void put_granule_pairs(rsrc_t rs, unsigned char* ws, unsigned off, float v, unsigned tag, int lane) {
  const float nb = dpp<XOR1>(v);
  if (!(lane & 1)) __builtin_amdgcn_raw_buffer_store_b128(u32x4{__float_as_uint(v), tag, __float_as_uint(nb), tag}, rs, off, 0, GST);
}
__device__ __forceinline__ u32x4 get_granules2(rsrc_t rs, unsigned off) {
  return __builtin_amdgcn_raw_buffer_load_b128(rs, off, 0, GLD);
}
__device__ __forceinline__ s16x4 tr_read(unsigned char* lds, int off) {
  return __builtin_amdgcn_ds_read_tr16_b64_v4i16((LAS s16x4*)(lds + off));
}
__device__ __forceinline__ f32x4 mfma32(s16x4 a0, s16x4 a1, s16x4 b0, s16x4 b1, f32x4 c) {
  const s16x8 a = __builtin_shufflevector(a0, a1, 0, 1, 2, 3, 4, 5, 6, 7), b = __builtin_shufflevector(b0, b1, 0, 1, 2, 3, 4, 5, 6, 7);
  return __builtin_amdgcn_mfma_f32_16x16x32_bf16(__builtin_bit_cast(bf16x8, a), __builtin_bit_cast(bf16x8, b), c, 0, 0, 0);
}

__device__ __forceinline__ float wave_sum(float x) {
  x += dpp<XOR1>(x); x += dpp<XOR2>(x); x += dpp<XOR7>(x); x += dpp<XOR8>(x);
  return x16_sum(x);
}
__device__ __forceinline__ void lds_set(unsigned char* lds, int flag) {
  asm volatile("s_waitcnt lgkmcnt(0)" ::: "memory");
  __hip_atomic_store((LAS unsigned*)(lds + L_FLAG) + flag, 1u, __ATOMIC_RELAXED, __HIP_MEMORY_SCOPE_WORKGROUP);
}
__device__ __forceinline__ void lds_wait(unsigned char* lds, int flag) {
#pragma unroll 1
  for (unsigned spins = 0; spins < (SPIN_CAP << 4); ++spins) {
    if (__all(__hip_atomic_load((LAS unsigned*)(lds + L_FLAG) + flag, __ATOMIC_RELAXED, __HIP_MEMORY_SCOPE_WORKGROUP) != 0u)) break;
    __builtin_amdgcn_s_sleep(1);
  }
  asm volatile("" ::: "memory");
}
__device__ __forceinline__ void dma16_asm(const void* base, unsigned ldsaddr, int voff) {
  const unsigned long long b = (unsigned long long)base;
  const u32x4 rs = {(unsigned)b, (unsigned)(b >> 32) & 0xffffu, 0x7fffffffu, 0x00020000u};
  unsigned keep;
  asm volatile("s_mov_b32 %0, m0\n\ts_mov_b32 m0, %1\n\ts_nop 4\n\tbuffer_load_dwordx4 %2, %3, 0 offen nt lds\n\ts_mov_b32 m0, %0"
               : "=&s"(keep) : "s"(ldsaddr), "v"(voff), "s"(rs) : "memory");
}
__device__ __forceinline__ void dma16(rsrc_t rs, unsigned char* lds, int ldsoff, int voff) {
  __builtin_amdgcn_raw_ptr_buffer_load_lds(rs, (LAS void*)(lds + ldsoff), 16, voff, 0, 0, AUX_NT);
}

__device__ __forceinline__ void dma16s(rsrc_t rs, unsigned char* lds, int ldsoff, int voff, int soff) {
  __builtin_amdgcn_raw_ptr_buffer_load_lds(rs, (LAS void*)(lds + ldsoff), 16, voff, soff, 0, AUX_NT);
}

#define ST(i) do {} while (0)
#define ST_END() do {} while (0)
#define PST(i) do {} while (0)
#define STV(i, v) do {} while (0)

__host__ __device__ constexpr int issued(int G) {
  int a = G < 0 ? RING : RING + (G >= 1 ? (G + CATCH - 1) / CATCH : 0), b = G + 1 + LEAD, n = a > b ? a : b;
  return n > 32 ? 32 : n;
}
__device__ __forceinline__ unsigned next_tag(unsigned epoch_v) {
  const unsigned t = __builtin_amdgcn_readfirstlane(epoch_v) + 1u;
  return t ? t : 1u;
}
template <int I> struct ic { static constexpr int value = I; };
template <int I, int N, class F> __device__ __forceinline__ void static_for(F&& f) {
  if constexpr (I < N) { f(ic<I>{}); static_for<I + 1, N>(f); }
}
struct Params {
  const u16 *x, *kc, *vc, *wq, *wo, *wg, *wu, *wd;
  u16* out;
  unsigned char* ws;
};

__global__ void __launch_bounds__(NTHR, 3) fused_decode(Params p) {
  __shared__ __attribute__((aligned(16))) unsigned char lds[L_END];
  const int tid = threadIdx.x, lane = tid & 63, w = __builtin_amdgcn_readfirstlane(tid >> 6);
  const int wg = blockIdx.x, idx = wg >> 3, h = (wg & 7) * 4 + (idx >> 3), j = idx & 7;
  const int g4 = lane >> 4, i16 = lane & 15, bl = (lane >> 2) & 3;
  const int tr_row = 4 * g4 + bl, tr_col = 8 * (lane & 3);
  const rsrc_t rws = mk_rsrc(p.ws, WS_END);
  const unsigned epoch_v = __hip_atomic_load((GAS unsigned*)p.ws, __ATOMIC_RELAXED, __HIP_MEMORY_SCOPE_AGENT);
  unsigned tag, nsweep = 0;
  unsigned stampv = 0;

  ST(0);
  unsigned xv[5], touch = 0;
  if (w < 4) {
#pragma unroll
    for (int s = 0; s < 5; ++s) xv[s] = *(const unsigned*)(p.x + w * DM + 128 * s + 2 * lane);
    const rsrc_t rq = mk_rsrc(p.wq + (size_t)(80 * j + 20 * w) * QN + h * DH, 20 * QN * 2);
    int cq[3];
#pragma unroll
    for (int i = 0; i < 3; ++i) { const int ci = i * 64 + lane, row = ci / 24; cq[i] = row * (QN * 2) + (ci - row * 24) * 16; }
#pragma unroll
    for (int i = 0; i < 8; ++i)
      if (i < 7 || lane < 32) dma16s(rq, lds, L_WQ + w * (20 * RS_WQ) + i * 1024, cq[i % 3], (i / 3) * (8 * QN * 2));
    bar();
  } else {
    const u16* bg = (j < 4 ? p.wg : p.wu) + (size_t)160 * (j & 3) * FF + 48 * h;
    const u16* bo = p.wo + (size_t)DH * h * DM + 80 * j;
    const u16* bd = p.wd + (size_t)48 * h * DM + 80 * j - 1920 / 10 * DM;
    if (tid < 288) *((LAS unsigned*)(lds + L_FLAG) + lane) = 0u;
    bar();
    ST(20);
#pragma unroll 1
    for (int pc = w - 4; pc < ((XP & 64) ? 0 : 53); pc += 8) {
      const bool gu = pc < 15;
      const int ci = (gu ? pc : pc - 15) * 64 + lane, row = (ci * (gu ? 10923 : 6554)) >> 16;
      const int vo = row * (gu ? FF * 2 - 96 : DM * 2 - 160) + ci * 16;
      if (pc < 52 || lane < 32) dma16_asm(gu ? bg : pc < 45 ? bo : bd, (unsigned)(size_t)(LAS unsigned char*)(lds + L_GU) + pc * 1024, vo);
    }
  }
  int wr = w;
  asm volatile("" : "+s"(wr));

  if (wr >= 4) {
    ST(18);
    const int sw = w - 4, r = lane >> 3, c = lane & 7;
    const size_t kvoff = ((size_t)((j >> 1) * NH + h) * SK + (SK - WIN) + (j & 1) * 1024) * DH;
    const rsrc_t rk = mk_rsrc(p.kc + kvoff, 1024 * DH * 2), rv = mk_rsrc(p.vc + kvoff, 1024 * DH * 2);
    const int voff = r * 384 + c * 16;
    const int rot = ROTM ? idx : 0;
    u32x4 ring[RING][3];
#define ISSUE(G)                                                                                              \
  do {                                                                                                        \
    const int so_ = (((((G) & 15) + rot) & 15) * 8 + sw) * 3072;                                              \
    _Pragma("unroll") for (int jj = 0; jj < 3; ++jj) ring[(G) % RING][jj] =                                   \
        __builtin_amdgcn_raw_buffer_load_b128((G) < 16 ? rk : rv, voff + jj * 128, so_, AUX_NT);              \
  } while (0)
    __builtin_amdgcn_sched_barrier(0);
    wait_vm<WQW>();
    ST(1);
    static_for<0, 3 * RING>([&](auto nc) {
      constexpr int N = decltype(nc)::value, G = N / 3;
      if constexpr (N >= FLY) wait_vm<FLY - 1>();
      if constexpr (N >= FLY && (N - FLY) % 3 == 2) PST((N - FLY) / 3);
      if constexpr (N == FLY) {
        tag = next_tag(epoch_v);
        lds_set(lds, 8 + sw);
        ST(21);
        ST(22);
      }
      ring[G][N % 3] = __builtin_amdgcn_raw_buffer_load_b128(rk, voff + (N % 3) * 128, (((G + rot) & 15) * 8 + sw) * 3072, AUX_NT);
      __builtin_amdgcn_sched_barrier(0);
    });
    ST(3);
    lds_wait(lds, 16 + (lane & 3));
    ST(4);
    u32x4 qv[3];
#pragma unroll
    for (int jj = 0; jj < 3; ++jj) qv[jj] = *(LAS u32x4*)(lds + L_Q + jj * 128 + c * 16);
#define STEP(G)                                                        \
  do {                                                                 \
    __builtin_amdgcn_sched_barrier(0);                                 \
    if constexpr (STAMP == 2 && (G) >= (3 * RING - FLY) / 3) { wait_vm<3 * (issued((G) - 1) - (G) - 1)>(); PST(G); } \
    if constexpr (issued(G) > issued((G) - 1)) {                                 \
      wait_vm<3 * (issued((G) - 1) - (G) - 1)>();                      \
      _Pragma("unroll") for (int N = issued((G) - 1); N < issued(G); ++N) ISSUE(N); \
    }                                                                  \
    __builtin_amdgcn_sched_barrier(0);                                 \
  } while (0)
    float sc[16];
    static_for<0, 16>([&](auto gc) {
      constexpr int G = decltype(gc)::value;
      STEP(G);
      float d0 = 0.f, d1 = 0.f, d2 = 0.f;
#pragma unroll
      for (int k = 0; k < 4; ++k) {
        d0 = dot2(ring[G % RING][0][k], qv[0][k], d0);
        d1 = dot2(ring[G % RING][1][k], qv[1][k], d1);
        d2 = dot2(ring[G % RING][2][k], qv[2][k], d2);
      }
      float d = (d0 + d1) + d2;
      d += dpp<XOR1>(d); d += dpp<XOR2>(d); d += dpp<XOR7>(d);
      sc[G] = rbf(rbf(d) * 0.07216878364870322f);
      asm volatile("" : "+v"(sc[G]));
    });
    ST(5);
    float m = sc[0];
#pragma unroll
    for (int G = 1; G < 16; ++G) m = fmaxf(m, sc[G]);
    m = fmaxf(m, dpp<XOR8>(m));
    m = x16_max(m);
    float l = 0.f;
#pragma unroll
    for (int G = 0; G < 16; ++G) { sc[G] = __builtin_amdgcn_exp2f((sc[G] - m) * LOG2E); l += sc[G]; }
    l += dpp<XOR8>(l);
    l = x16_sum(l);
    f32x2 acc[12];
#pragma unroll
    for (int k = 0; k < 12; ++k) acc[k] = f32x2{0.f, 0.f};
    static_for<16, 32>([&](auto gc) {
      constexpr int G = decltype(gc)::value;
      STEP(G);
      const f32x2 pp = {sc[G - 16], sc[G - 16]};
#pragma unroll
      for (int jj = 0; jj < 3; ++jj)
#pragma unroll
        for (int k = 0; k < 4; ++k) {
          const unsigned u = ring[G % RING][jj][k];
          if (XP & 4) { if (k == 0) acc[jj * 4][0] += __uint_as_float(u ^ ring[G % RING][jj][1] ^ ring[G % RING][jj][2] ^ ring[G % RING][jj][3]); }
          else acc[jj * 4 + k] = __builtin_elementwise_fma(f32x2{bf_lo(u), bf_hi(u)}, pp, acc[jj * 4 + k]);
        }
    });
#undef STEP
#undef ISSUE
    ST(6);
    ST(17);
    float t12[12], t6[6];
#pragma unroll
    for (int k = 0; k < 12; ++k) t12[k] = swap32_add(acc[k >> 1][k & 1], acc[(k + 12) >> 1][k & 1]);
#pragma unroll
    for (int k = 0; k < 6; ++k) t6[k] = swap16_add(t12[k], t12[k + 6]);
    float* mrg = (float*)(lds + L_MRG) + sw * 200;
#pragma unroll
    for (int k = 0; k < 3; ++k) {
      const float a = t6[k] + dpp<XOR8>(t6[k]), b2 = t6[k + 3] + dpp<XOR8>(t6[k + 3]);
      const int e = 3 * r + k;
      mrg[(e >> 3) * 64 + c * 8 + (e & 7)] = (lane & 8) ? b2 : a;
    }
    if (lane == 0) { mrg[192] = m; mrg[193] = l; }
  } else {
    __builtin_amdgcn_s_setprio(3);
    {
      float s1 = 0.f;
#pragma unroll
      for (int s = 0; s < 5; ++s) s1 += bf_lo(xv[s]) + bf_hi(xv[s]);
      const float mean = wave_sum(s1) * (1.f / DM);
      float s2 = 0.f;
#pragma unroll
      for (int s = 0; s < 5; ++s) {
        const float a = bf_lo(xv[s]) - mean, c = bf_hi(xv[s]) - mean;
        s2 = fmaf(a, a, s2); s2 = fmaf(c, c, s2);
      }
      const float mb = rbf(mean), rs = __builtin_amdgcn_rcpf(rbf(sqrtf(rbf(rbf(wave_sum(s2) * (1.f / DM)) + 1e-5f))));
#pragma unroll
      for (int s = 0; s < 5; ++s)
        *(LAS unsigned*)(lds + L_X + w * RS_X + 256 * s + 4 * lane) = cvtpk(rbf(bf_lo(xv[s]) - mb) * rs, rbf(bf_hi(xv[s]) - mb) * rs);
      tag = next_tag(epoch_v);
      ST(7);
      wait_vm<0>();
      asm volatile("" :: "v"(touch));
      lds_set(lds, 1 + w);
      lds_wait(lds, 1 + (lane & 3));
      s16x4 af[5];
#pragma unroll
      for (int s = 0; s < 5; ++s) af[s] = *(LAS s16x4*)(lds + L_X + bl * RS_X + (80 * j + 16 * s + 4 * g4) * 2);
#pragma unroll
      for (int t = 0; t < 3; ++t) {
        const int n0 = 48 * w + 16 * t;
        s16x4 bf[5];
#pragma unroll
        for (int s = 0; s < 5; ++s) bf[s] = tr_read(lds, L_WQ + (16 * s + tr_row) * RS_WQ + n0 * 2 + tr_col);
        f32x4 d = mfma32(af[0], af[1], bf[0], bf[1], f32x4{0.f, 0.f, 0.f, 0.f});
        d = mfma32(af[2], af[3], bf[2], bf[3], d);
        d = mfma32(af[4], s16x4{0, 0, 0, 0}, bf[4], bf[4], d);
        if (!(XP & 16) || d[0] == 123.456f) put_granule_pairs(rws, p.ws, WS_QP + (((h * 8 + j) * 4 + g4) * DH + n0 + i16) * 8, d[0], tag, lane);
      }
      ST(2);
    }
    {
      const unsigned o0 = WS_QP + (((h * 8 + (lane >> 3)) * 4 + (j >> 1)) * DH + 48 * w) * 8 + (lane & 7) * 16;
      u32x4 a[3];
      bool ok = false;
      __builtin_amdgcn_s_sleep(SLQ0);
#pragma unroll 1
      for (unsigned spins = 0; spins < SPIN_CAP; ++spins) {
        asm volatile("" ::: "memory");
        if (XP & 32) { a[0] = a[1] = a[2] = u32x4{0, 0, 0, 0}; } else
        if (!ok) {
#pragma unroll
          for (int i = 0; i < 3; ++i) a[i] = get_granules2(rws, o0 + i * 128);
          ok = true;
#pragma unroll
          for (int i = 0; i < 3; ++i) ok &= (a[i][1] == tag) & (a[i][3] == tag);
        }
        ++nsweep;
        if (__all(ok) || (XP & 48)) break;
        __builtin_amdgcn_s_sleep(SLQ);
      }
#pragma unroll
      for (int i = 0; i < 3; ++i) {
        float s0 = __uint_as_float(a[i][0]), s1 = __uint_as_float(a[i][2]);
        s0 += dpp<XOR8>(s0); s1 += dpp<XOR8>(s1);
        s0 = x16_sum(s0); s1 = x16_sum(s1);
        if (lane < 8) *(LAS unsigned*)(lds + L_Q + (24 * w + 8 * i + lane) * 4) = cvtpk(s0, s1);
      }
      lds_set(lds, 16 + w);
      ST(8);
      STV(19, nsweep);
    }
    if (w == 1 && !(XP & 1)) {
      const int jk = j & 3;
      lds_wait(lds, 8 + (lane & 7));
      ST(9);
      s16x4 af[10];
#pragma unroll
      for (int s = 0; s < 10; ++s) af[s] = *(LAS s16x4*)(lds + L_X + bl * RS_X + (160 * jk + 16 * s + 4 * g4) * 2);
#pragma unroll
      for (int t = 0; t < 3; ++t) {
        f32x4 d = {0.f, 0.f, 0.f, 0.f};
#pragma unroll
        for (int s = 0; s < 10; s += 2) {
          const s16x4 b0 = tr_read(lds, L_GU + (16 * s + tr_row) * RS_GU + 32 * t + tr_col);
          const s16x4 b1 = tr_read(lds, L_GU + (16 * s + 16 + tr_row) * RS_GU + 32 * t + tr_col);
          d = mfma32(af[s], af[s + 1], b0, b1, d);
        }
        put_granule_pairs(rws, p.ws, WS_GU + (((h * 8 + j) * 4 + g4) * 48 + 16 * t + i16) * 8, d[0], tag, lane);
      }
      ST(10);
      __builtin_amdgcn_s_sleep(64);
    }
    if (w == 1 && !(XP & 1)) {
#pragma unroll
      for (int pass = 0; pass < 2; ++pass) {
        const int it = pass == 0 ? lane : 64 + (lane & 31), bb = it / 24, pr = it - bb * 24;
        const unsigned o0 = WS_GU + ((h * 8 * 4 + bb) * 48 + 2 * pr) * 8;
        u32x4 a[8];
#pragma unroll 1
        for (unsigned spins = 0; spins < SPIN_CAP; ++spins) {
          asm volatile("" ::: "memory");
#pragma unroll
          for (int i = 0; i < 8; ++i) a[i] = get_granules2(rws, o0 + i * (192 * 8));
          bool ok = true;
#pragma unroll
          for (int i = 0; i < 8; ++i) ok &= (a[i][1] == tag) & (a[i][3] == tag);
          if (__all(ok)) break;
          __builtin_amdgcn_s_sleep(32);
        }
        float hv[2];
#pragma unroll
        for (int e = 0; e < 2; ++e) {
          const float gg = rbf((__uint_as_float(a[0][2 * e]) + __uint_as_float(a[1][2 * e])) + (__uint_as_float(a[2][2 * e]) + __uint_as_float(a[3][2 * e])));
          const float uu = rbf((__uint_as_float(a[4][2 * e]) + __uint_as_float(a[5][2 * e])) + (__uint_as_float(a[6][2 * e]) + __uint_as_float(a[7][2 * e])));
          const float inner = 0.7978845608028654f * (gg + 0.044715f * gg * gg * gg);
          hv[e] = rbf(0.5f * gg * (1.f + tanhf(inner))) * uu;
        }
        *(LAS unsigned*)(lds + L_O + bb * RS_O + (DH + 2 * pr) * 2) = cvtpk(hv[0], hv[1]);
      }
    }
    ST(11);
  }

  bar();
  ST(12);
  const float* mrg = (const float*)(lds + L_MRG);
  if (w < 4) {
    float M = mrg[192];
#pragma unroll
    for (int i = 1; i < 8; ++i) M = fmaxf(M, mrg[i * 200 + 192]);
    const int d = w < 3 ? tid : 193;
    float o = 0.f;
#pragma unroll
    for (int i = 0; i < 8; ++i) o = fmaf(__builtin_amdgcn_exp2f((mrg[i * 200 + 192] - M) * LOG2E), mrg[i * 200 + d], o);
    if (w < 3) put_granule_pairs(rws, p.ws, WS_OP + ((h * 8 + j) * 208 + tid) * 8, o, tag, lane);
    else if (lane == 0) __builtin_amdgcn_raw_buffer_store_b128(u32x4{__float_as_uint(M), tag, __float_as_uint(o), tag}, rws, WS_OP + ((h * 8 + j) * 208 + 192) * 8, 0, AUX_SC1);
  }
  s16x4 wf[15];
  if (w < 5) {
#pragma unroll
    for (int s = 0; s < 15; ++s) wf[s] = tr_read(lds, L_WO + (16 * s + tr_row) * RS_WO + 32 * w + tr_col);
  }
  ST(13);
  if (w < 6) {
    const int bb = tid / 96, pr = tid - bb * 96;
    const unsigned ob = WS_OP + (h * 8 + 2 * bb) * 208 * 8;
    u32x4 a0, a1, m0, m1;
    bool ok = false;
#pragma unroll 1
    for (unsigned spins = 0; spins < SPIN_CAP; ++spins) {
      asm volatile("" ::: "memory");
      if (!ok) {
        a0 = get_granules2(rws, ob + pr * 16);
        a1 = get_granules2(rws, ob + 208 * 8 + pr * 16);
        m0 = get_granules2(rws, ob + 192 * 8);
        m1 = get_granules2(rws, ob + 208 * 8 + 192 * 8);
        ok = (a0[1] == tag) & (a0[3] == tag) & (a1[1] == tag) & (a1[3] == tag) & (m0[1] == tag) & (m0[3] == tag) & (m1[1] == tag) & (m1[3] == tag);
      }
      if (__all(ok)) break;
      __builtin_amdgcn_s_sleep(SL1);
    }
    const float ma = __uint_as_float(m0[0]), mb2 = __uint_as_float(m1[0]), M = fmaxf(ma, mb2);
    const float wa = __builtin_amdgcn_exp2f((ma - M) * LOG2E), wb = __builtin_amdgcn_exp2f((mb2 - M) * LOG2E);
    const float inv = __builtin_amdgcn_rcpf(fmaf(wa, __uint_as_float(m0[2]), wb * __uint_as_float(m1[2])));
    const float ua = wa * inv, ub = wb * inv;
    *(LAS unsigned*)(lds + L_O + bb * RS_O + pr * 4) =
        cvtpk(fmaf(ua, __uint_as_float(a0[0]), ub * __uint_as_float(a1[0])), fmaf(ua, __uint_as_float(a0[2]), ub * __uint_as_float(a1[2])));
  }
  ST(14);
  bar();
  if (w < 5) {
    s16x4 of[15];
#pragma unroll
    for (int s = 0; s < 15; ++s) of[s] = *(LAS s16x4*)(lds + L_O + bl * RS_O + (16 * s + 4 * g4) * 2);
    f32x4 d0 = {0.f, 0.f, 0.f, 0.f}, d1 = d0;
#pragma unroll
    for (int s = 0; s < 8; s += 2) d0 = mfma32(of[s], of[s + 1], wf[s], wf[s + 1], d0);
#pragma unroll
    for (int s = 8; s < 14; s += 2) d1 = mfma32(of[s], of[s + 1], wf[s], wf[s + 1], d1);
    d1 = mfma32(of[14], s16x4{0, 0, 0, 0}, wf[14], wf[14], d1);
    put_granule_pairs(rws, p.ws, WS_YP + (((j * 20 + 4 * w + g4) * 32 + h) * 16 + i16) * 8, d0[0] + d1[0], tag, lane);
    ST(15);
  } else if (w == 6 && h < 20) {
    const int oi = (h & 3) * DM + 80 * j + 16 * (h >> 2) + 2 * (lane & 7);
    const unsigned xr = *(const unsigned*)(p.x + oi);
    const unsigned yb = WS_YP + (j * 20 + h) * 512 * 8 + lane * 16;
    u32x4 a[4];
    bool ok[4] = {false, false, false, false};
#pragma unroll 1
    for (unsigned spins = 0; spins < SPIN_CAP; ++spins) {
      asm volatile("" ::: "memory");
#pragma unroll
      for (int i = 0; i < 4; ++i)
        if (!ok[i]) a[i] = get_granules2(rws, yb + i * 1024);
#pragma unroll
      for (int i = 0; i < 4; ++i) ok[i] = (a[i][1] == tag) & (a[i][3] == tag);
      if (__all(ok[0] & ok[1] & ok[2] & ok[3])) break;
      __builtin_amdgcn_s_sleep(SL2);
    }
    float s0 = (__uint_as_float(a[0][0]) + __uint_as_float(a[1][0])) + (__uint_as_float(a[2][0]) + __uint_as_float(a[3][0]));
    float s1 = (__uint_as_float(a[0][2]) + __uint_as_float(a[1][2])) + (__uint_as_float(a[2][2]) + __uint_as_float(a[3][2]));
    s0 += dpp<XOR8>(s0); s1 += dpp<XOR8>(s1);
    s0 = x16_sum(s0); s1 = x16_sum(s1);
    if (lane < 8) *(unsigned*)(p.out + oi) = cvtpk(bf_lo(xr) + s0, bf_hi(xr) + s1);
    if (wg == 0 && lane == 0) __hip_atomic_store((GAS unsigned*)p.ws, tag, __ATOMIC_RELAXED, __HIP_MEMORY_SCOPE_AGENT);
    ST(16);
  }
  ST_END();
}

extern "C" void kernel_launch(void* const* d_in, const int* in_sizes, int n_in, void* d_out, int out_size, void* d_ws,
                              size_t ws_size, hipStream_t stream) {
  Params p;
  p.x = (const u16*)d_in[0];
  p.kc = (const u16*)d_in[1];
  p.vc = (const u16*)d_in[2];
  p.wq = (const u16*)d_in[3];
  p.wo = (const u16*)d_in[4];
  p.wg = (const u16*)d_in[6];
  p.wu = (const u16*)d_in[7];
  p.wd = (const u16*)d_in[8];
  p.out = (u16*)d_out;
  p.ws = (unsigned char*)d_ws + (WSPOS == 0 ? 0 : WSPOS == 1 ? ((ws_size - WS_END) & ~(size_t)4095) : ((ws_size - WS_END - ((size_t)WSPOS << 20)) & ~(size_t)4095));
  fused_decode<<<256, NTHR, 0, stream>>>(p);
}
```

```cpp
#include <hip/hip_runtime.h>
#include <hip/hip_bf16.h>
#include <stdint.h>

#define RING 7
#define W0 2
#define LEAD 2
#define CATCH 4
#define WQW 4
#define ROTM 1
#define CODELEN 21504
#define XP 0
#define SL1 4
#define SL2 4
#define CODEPF 0
#define PAIRS 1
#define QMASK 1
#define SLQ 8
#define SLQ0 8
#define GLD 16
#define GST 16
#define TOUCH 0
#define FLY 4
#define WSPOS 0
#define GUL 2
#define ROTQ 0
#define ROTG 2
#define GE 99
#define LEADE 4
#define TS 0
#define TA 315
#define STAMP 0

typedef unsigned short u16;
typedef unsigned u32x2 __attribute__((ext_vector_type(2)));
typedef unsigned u32x4 __attribute__((ext_vector_type(4)));
typedef float f32x2 __attribute__((ext_vector_type(2)));
typedef float f32x4 __attribute__((ext_vector_type(4)));
typedef short s16x4 __attribute__((ext_vector_type(4)));
typedef short s16x8 __attribute__((ext_vector_type(8)));
typedef __bf16 bf16x2 __attribute__((ext_vector_type(2)));
typedef __bf16 bf16x8 __attribute__((ext_vector_type(8)));
#define LAS __attribute__((address_space(3)))
#define GAS __attribute__((address_space(1)))
typedef __amdgpu_buffer_rsrc_t rsrc_t;

constexpr int DM = 640, NH = 32, DH = 192, SK = 8192, WIN = 2048, FF = 1536, QN = NH * DH;
constexpr int NTHR = 768;
constexpr float LOG2E = 1.4426950408889634f;
constexpr unsigned SPIN_CAP = 1u << 18;

constexpr unsigned WS_QP = 4096;
constexpr unsigned WS_GU = WS_QP + 32 * 4 * 96 * 8;
constexpr unsigned WS_OP = WS_GU + 8 * 2 * 4 * 96 * 8;
constexpr unsigned WS_YP = WS_OP + 32 * 8 * 208 * 8;
constexpr unsigned WS_END = WS_YP + 8 * 20 * 32 * 16 * 8;

constexpr int RS_WO = 160, RS_O = 496;
constexpr int L_W1 = 0;
constexpr int L_WO = L_W1 + 640 * 128;
constexpr int L_MRG = L_WO + 240 * RS_WO + 512;
constexpr int L_Q = L_MRG + 8 * 800;
constexpr int L_O = L_Q + 384;
constexpr int RS_X = 1296;
constexpr int L_X = L_O + 4 * RS_O;
constexpr int L_FLAG = L_X + 4 * RS_X;
constexpr int L_RED = L_FLAG + 128;
constexpr int L_END = L_RED + 4096 + 512;

__device__ __forceinline__ float bf_lo(unsigned u) { return __uint_as_float(u << 16); }
__device__ __forceinline__ float bf_hi(unsigned u) { return __uint_as_float(u & 0xffff0000u); }
__device__ __forceinline__ unsigned cvtpk(float lo, float hi) {
  bf16x2 r = {(__bf16)lo, (__bf16)hi};
  return __builtin_bit_cast(unsigned, r);
}
__device__ __forceinline__ float rbf(float x) { return bf_lo(cvtpk(x, 0.f)); }
__device__ __forceinline__ float dot2(unsigned k, unsigned q, float acc) {
  return __builtin_amdgcn_fdot2_f32_bf16(__builtin_bit_cast(bf16x2, k), __builtin_bit_cast(bf16x2, q), acc, false);
}
template <int CTRL> __device__ __forceinline__ float dpp(float x) {
  return __builtin_bit_cast(float, __builtin_amdgcn_mov_dpp(__builtin_bit_cast(int, x), CTRL, 0xf, 0xf, true));
}
constexpr int XOR1 = 0xB1, XOR2 = 0x4E, XOR7 = 0x141, XOR8 = 0x128;
__device__ __forceinline__ float swap16_add(float a, float b) {
  auto s = __builtin_amdgcn_permlane16_swap(__float_as_uint(a), __float_as_uint(b), false, false);
  return __uint_as_float(s[0]) + __uint_as_float(s[1]);
}
__device__ __forceinline__ float swap32_add(float a, float b) {
  auto s = __builtin_amdgcn_permlane32_swap(__float_as_uint(a), __float_as_uint(b), false, false);
  return __uint_as_float(s[0]) + __uint_as_float(s[1]);
}
__device__ __forceinline__ float x16_sum(float x) { x = swap16_add(x, x); return swap32_add(x, x); }
__device__ __forceinline__ float x16_max(float x) {
  auto s = __builtin_amdgcn_permlane16_swap(__float_as_uint(x), __float_as_uint(x), false, false);
  x = fmaxf(__uint_as_float(s[0]), __uint_as_float(s[1]));
  auto t = __builtin_amdgcn_permlane32_swap(__float_as_uint(x), __float_as_uint(x), false, false);
  return fmaxf(__uint_as_float(t[0]), __uint_as_float(t[1]));
}
__device__ __forceinline__ void bar() { asm volatile("s_waitcnt lgkmcnt(0)\n\ts_barrier" ::: "memory"); }
template <int N> __device__ __forceinline__ void wait_vm() { asm volatile("s_waitcnt vmcnt(%0)" ::"n"(N) : "memory"); }
__device__ __forceinline__ rsrc_t mk_rsrc(const void* p, unsigned bytes) {
  return __builtin_amdgcn_make_buffer_rsrc((void*)p, 0, bytes, 0x00020000);
}
constexpr int AUX_NT = 2, AUX_SC1 = 16;
__device__ __forceinline__ void put_granule(unsigned char* ws, unsigned off, float v, unsigned tag) {
  __hip_atomic_store((GAS unsigned long long*)(ws + off), ((unsigned long long)tag << 32) | __float_as_uint(v), __ATOMIC_RELAXED,
                     __HIP_MEMORY_SCOPE_AGENT);
}
__device__ __forceinline__ void put_granule_pairs(rsrc_t rs, unsigned char* ws, unsigned off, float v, unsigned tag, int lane) {
  const float nb = dpp<XOR1>(v);
  if (!(lane & 1)) __builtin_amdgcn_raw_buffer_store_b128(u32x4{__float_as_uint(v), tag, __float_as_uint(nb), tag}, rs, off, 0, GST);
}
__device__ __forceinline__ u32x4 get_granules2(rsrc_t rs, unsigned off) {
  return __builtin_amdgcn_raw_buffer_load_b128(rs, off, 0, GLD);
}
__device__ __forceinline__ s16x4 tr_read(unsigned char* lds, int off) {
  return __builtin_amdgcn_ds_read_tr16_b64_v4i16((LAS s16x4*)(lds + off));
}
__device__ __forceinline__ f32x4 mfma32(s16x4 a0, s16x4 a1, s16x4 b0, s16x4 b1, f32x4 c) {
  const s16x8 a = __builtin_shufflevector(a0, a1, 0, 1, 2, 3, 4, 5, 6, 7), b = __builtin_shufflevector(b0, b1, 0, 1, 2, 3, 4, 5, 6, 7);
  return __builtin_amdgcn_mfma_f32_16x16x32_bf16(__builtin_bit_cast(bf16x8, a), __builtin_bit_cast(bf16x8, b), c, 0, 0, 0);
}

__device__ __forceinline__ float wave_sum(float x) {
  x += dpp<XOR1>(x); x += dpp<XOR2>(x); x += dpp<XOR7>(x); x += dpp<XOR8>(x);
  return x16_sum(x);
}
__device__ __forceinline__ void lds_set(unsigned char* lds, int flag) {
  asm volatile("s_waitcnt lgkmcnt(0)" ::: "memory");
  __hip_atomic_store((LAS unsigned*)(lds + L_FLAG) + flag, 1u, __ATOMIC_RELAXED, __HIP_MEMORY_SCOPE_WORKGROUP);
}
__device__ __forceinline__ void lds_wait(unsigned char* lds, int flag) {
#pragma unroll 1
  for (unsigned spins = 0; spins < (SPIN_CAP << 4); ++spins) {
    if (__all(__hip_atomic_load((LAS unsigned*)(lds + L_FLAG) + flag, __ATOMIC_RELAXED, __HIP_MEMORY_SCOPE_WORKGROUP) != 0u)) break;
    __builtin_amdgcn_s_sleep(1);
  }
  asm volatile("" ::: "memory");
}
__device__ __forceinline__ void dma16_asm(const void* base, unsigned ldsaddr, int voff) {
  const unsigned long long b = (unsigned long long)base;
  const u32x4 rs = {(unsigned)b, (unsigned)(b >> 32) & 0xffffu, 0x7fffffffu, 0x00020000u};
  unsigned keep;
  asm volatile("s_mov_b32 %0, m0\n\ts_mov_b32 m0, %1\n\ts_nop 4\n\tbuffer_load_dwordx4 %2, %3, 0 offen nt lds\n\ts_mov_b32 m0, %0"
               : "=&s"(keep) : "s"(ldsaddr), "v"(voff), "s"(rs) : "memory");
}
__device__ __forceinline__ void dma16(rsrc_t rs, unsigned char* lds, int ldsoff, int voff) {
  __builtin_amdgcn_raw_ptr_buffer_load_lds(rs, (LAS void*)(lds + ldsoff), 16, voff, 0, 0, AUX_NT);
}

__device__ __forceinline__ void dma16s(rsrc_t rs, unsigned char* lds, int ldsoff, int voff, int soff) {
  __builtin_amdgcn_raw_ptr_buffer_load_lds(rs, (LAS void*)(lds + ldsoff), 16, voff, soff, 0, AUX_NT);
}

#define ST(i) do {} while (0)
#define ST_END() do {} while (0)
#define PST(i) do {} while (0)
#define STV(i, v) do {} while (0)

__host__ __device__ constexpr int issued(int G) {
  int a = G < 0 ? RING : RING + (G >= 1 ? (G + CATCH - 1) / CATCH : 0), b = G + 1 + (G >= GE ? LEADE : LEAD), n = a > b ? a : b;
  return n > 32 ? 32 : n;
}
__device__ __forceinline__ void wait_until(unsigned t) {
  asm volatile("1:\n\ts_memrealtime vcc\n\ts_waitcnt lgkmcnt(0)\n\ts_sub_u32 vcc_lo, vcc_lo, %0\n\ts_cmp_lt_i32 vcc_lo, 0\n\t"
               "s_cbranch_scc0 2f\n\ts_sleep 1\n\ts_branch 1b\n2:" ::"s"(t) : "vcc", "scc");
}
__device__ __forceinline__ unsigned next_tag(unsigned epoch_v) {
  const unsigned t = __builtin_amdgcn_readfirstlane(epoch_v) + 1u;
  return t ? t : 1u;
}
template <int I> struct ic { static constexpr int value = I; };
template <int I, int N, class F> __device__ __forceinline__ void static_for(F&& f) {
  if constexpr (I < N) { f(ic<I>{}); static_for<I + 1, N>(f); }
}
struct Params {
  const u16 *x, *kc, *vc, *wq, *wo, *wg, *wu, *wd;
  u16* out;
  unsigned char* ws;
};

__global__ void __launch_bounds__(NTHR, 3) fused_decode(Params p) {
  __shared__ __attribute__((aligned(16))) unsigned char lds[L_END];
  const int tid = threadIdx.x, lane = tid & 63, w = __builtin_amdgcn_readfirstlane(tid >> 6);
  const int wg = blockIdx.x, idx = wg >> 3, h = (wg & 7) * 4 + (idx >> 3), j = idx & 7;
  const int g4 = lane >> 4, i16 = lane & 15, bl = (lane >> 2) & 3;
  const int tr_row = 4 * g4 + bl, tr_col = 8 * (lane & 3);
  const rsrc_t rws = mk_rsrc(p.ws, WS_END);
  const unsigned epoch_v = __hip_atomic_load((GAS unsigned*)p.ws, __ATOMIC_RELAXED, __HIP_MEMORY_SCOPE_AGENT);
  unsigned tag, nsweep = 0;
  unsigned stampv = 0;

  ST(0);
  const unsigned tstart = TS ? (unsigned)__builtin_amdgcn_s_memrealtime() : 0u;
  const int f = (idx >> 3) * 2 + j - 6, nt = j < 6 ? 2 : f < 6 ? 4 : 0;
  unsigned xv[5];
  if (w < 4) {
    const u16* xr = p.x + w * DM;
    asm volatile("global_load_dword %0, %5, %6\n\tglobal_load_dword %1, %5, %6 offset:256\n\tglobal_load_dword %2, %5, %6 offset:512\n\t"
                 "global_load_dword %3, %5, %6 offset:768\n\tglobal_load_dword %4, %5, %6 offset:1024"
                 : "=&v"(xv[0]), "=&v"(xv[1]), "=&v"(xv[2]), "=&v"(xv[3]), "=&v"(xv[4]) : "v"(lane * 4), "s"(xr) : "memory");
    const u16* bw = j < 6 ? p.wq + (size_t)160 * w * QN + h * DH + 32 * j
                          : (f < 3 ? p.wg : p.wu) + (size_t)160 * w * FF + 192 * (wg & 7) + 64 * (f < 3 ? f : f - 3);
    const int sh = j < 6 ? 2 : 3, gs = (j < 6 ? QN * 2 : FF * 2) << (6 - sh);
    int vo = (lane >> sh) * (j < 6 ? QN * 2 : FF * 2) + (lane & ((1 << sh) - 1)) * 16;
#pragma unroll 1
    for (int i = 0; i < 5 * nt; ++i, vo += gs) dma16_asm(bw, (unsigned)(size_t)(LAS unsigned char*)(lds + L_W1) + (w * 5 * nt + i) * 1024, vo);
    bar();
  } else {
    const u16* bo = p.wo + (size_t)DH * h * DM + 80 * j;
    const u16* bd = p.wd + (size_t)48 * h * DM + 80 * j - 1920 / 10 * DM;
    if (tid < 288) *((LAS unsigned*)(lds + L_FLAG) + lane) = 0u;
    bar();
    ST(20);
#pragma unroll 1
    for (int pc = w - 4; pc < 38; pc += 8) {
      const int ci = pc * 64 + lane, row = (ci * 6554) >> 16;
      if (pc < 37 || lane < 32) dma16_asm(pc < 30 ? bo : bd, (unsigned)(size_t)(LAS unsigned char*)(lds + L_WO) + pc * 1024, row * (DM * 2 - 160) + ci * 16);
    }
  }
  int wr = w;
  asm volatile("" : "+s"(wr));

  if (wr >= 4) {
    ST(18);
    const int sw = w - 4, r = lane >> 3, c = lane & 7;
    const size_t kvoff = ((size_t)((j >> 1) * NH + h) * SK + (SK - WIN) + (j & 1) * 1024) * DH;
    const rsrc_t rk = mk_rsrc(p.kc + kvoff, 1024 * DH * 2), rv = mk_rsrc(p.vc + kvoff, 1024 * DH * 2);
    const int voff = r * 384 + c * 16;
    const int rot = ROTM ? idx : 0;
    u32x4 ring[RING][3];
#define ISSUE(G)                                                                                              \
  do {                                                                                                        \
    const int so_ = (((((G) & 15) + rot) & 15) * 8 + sw) * 3072;                                              \
    _Pragma("unroll") for (int jj = 0; jj < 3; ++jj) ring[(G) % RING][jj] =                                   \
        __builtin_amdgcn_raw_buffer_load_b128((G) < 16 ? rk : rv, voff + jj * 128, so_, AUX_NT);              \
  } while (0)
    __builtin_amdgcn_sched_barrier(0);
    wait_vm<WQW>();
    ST(1);
    static_for<0, 3 * RING>([&](auto nc) {
      constexpr int N = decltype(nc)::value, G = N / 3;
      if constexpr (N >= FLY) wait_vm<FLY - 1>();
      if constexpr (N >= FLY && (N - FLY) % 3 == 2) PST((N - FLY) / 3);
      if constexpr (N == FLY) {
        tag = next_tag(epoch_v);
        ST(21);
        ST(22);
      }
      ring[G][N % 3] = __builtin_amdgcn_raw_buffer_load_b128(rk, voff + (N % 3) * 128, (((G + rot) & 15) * 8 + sw) * 3072, AUX_NT);
      __builtin_amdgcn_sched_barrier(0);
    });
    ST(3);
    lds_wait(lds, 16);
    ST(4);
    u32x4 qv[3];
#pragma unroll
    for (int jj = 0; jj < 3; ++jj) qv[jj] = *(LAS u32x4*)(lds + L_Q + jj * 128 + c * 16);
#define STEP(G)                                                        \
  do {                                                                 \
    __builtin_amdgcn_sched_barrier(0);                                 \
    if constexpr (STAMP == 2 && (G) >= (3 * RING - FLY) / 3) { wait_vm<3 * (issued((G) - 1) - (G) - 1)>(); PST(G); } \
    if constexpr (issued(G) > issued((G) - 1)) {                                 \
      wait_vm<3 * (issued((G) - 1) - (G) - 1)>();                      \
      if (TS) wait_until(tstart + TA + issued((G) - 1) * TS);          \
      _Pragma("unroll") for (int N = issued((G) - 1); N < issued(G); ++N) ISSUE(N); \
    }                                                                  \
    __builtin_amdgcn_sched_barrier(0);                                 \
  } while (0)
    float sc[16];
    static_for<0, 16>([&](auto gc) {
      constexpr int G = decltype(gc)::value;
      STEP(G);
      float d0 = 0.f, d1 = 0.f, d2 = 0.f;
#pragma unroll
      for (int k = 0; k < 4; ++k) {
        d0 = dot2(ring[G % RING][0][k], qv[0][k], d0);
        d1 = dot2(ring[G % RING][1][k], qv[1][k], d1);
        d2 = dot2(ring[G % RING][2][k], qv[2][k], d2);
      }
      float d = (d0 + d1) + d2;
      d += dpp<XOR1>(d); d += dpp<XOR2>(d); d += dpp<XOR7>(d);
      sc[G] = rbf(rbf(d) * 0.07216878364870322f);
      asm volatile("" : "+v"(sc[G]));
    });
    ST(5);
    float m = sc[0];
#pragma unroll
    for (int G = 1; G < 16; ++G) m = fmaxf(m, sc[G]);
    m = fmaxf(m, dpp<XOR8>(m));
    m = x16_max(m);
    float l = 0.f;
#pragma unroll
    for (int G = 0; G < 16; ++G) { sc[G] = __builtin_amdgcn_exp2f((sc[G] - m) * LOG2E); l += sc[G]; }
    l += dpp<XOR8>(l);
    l = x16_sum(l);
    f32x2 acc[12];
#pragma unroll
    for (int k = 0; k < 12; ++k) acc[k] = f32x2{0.f, 0.f};
    static_for<16, 32>([&](auto gc) {
      constexpr int G = decltype(gc)::value;
      STEP(G);
      const f32x2 pp = {sc[G - 16], sc[G - 16]};
#pragma unroll
      for (int jj = 0; jj < 3; ++jj)
#pragma unroll
        for (int k = 0; k < 4; ++k) {
          const unsigned u = ring[G % RING][jj][k];
          if (XP & 4) { if (k == 0) acc[jj * 4][0] += __uint_as_float(u ^ ring[G % RING][jj][1] ^ ring[G % RING][jj][2] ^ ring[G % RING][jj][3]); }
          else acc[jj * 4 + k] = __builtin_elementwise_fma(f32x2{bf_lo(u), bf_hi(u)}, pp, acc[jj * 4 + k]);
        }
    });
#undef STEP
#undef ISSUE
    ST(6);
    ST(17);
    float t12[12], t6[6];
#pragma unroll
    for (int k = 0; k < 12; ++k) t12[k] = swap32_add(acc[k >> 1][k & 1], acc[(k + 12) >> 1][k & 1]);
#pragma unroll
    for (int k = 0; k < 6; ++k) t6[k] = swap16_add(t12[k], t12[k + 6]);
    float* mrg = (float*)(lds + L_MRG) + sw * 200;
#pragma unroll
    for (int k = 0; k < 3; ++k) {
      const float a = t6[k] + dpp<XOR8>(t6[k]), b2 = t6[k + 3] + dpp<XOR8>(t6[k + 3]);
      const int e = 3 * r + k;
      mrg[(e >> 3) * 64 + c * 8 + (e & 7)] = (lane & 8) ? b2 : a;
    }
    if (lane == 0) { mrg[192] = m; mrg[193] = l; }
  } else {
    __builtin_amdgcn_s_setprio(3);
    {
      if (nt == 0) wait_vm<0>();
      asm volatile("s_waitcnt vmcnt(10)" : "+v"(xv[0]), "+v"(xv[1]), "+v"(xv[2]), "+v"(xv[3]), "+v"(xv[4]));
      float s1 = 0.f;
#pragma unroll
      for (int s = 0; s < 5; ++s) s1 += bf_lo(xv[s]) + bf_hi(xv[s]);
      const float mean = wave_sum(s1) * (1.f / DM);
      float s2 = 0.f;
#pragma unroll
      for (int s = 0; s < 5; ++s) {
        const float a = bf_lo(xv[s]) - mean, c = bf_hi(xv[s]) - mean;
        s2 = fmaf(a, a, s2); s2 = fmaf(c, c, s2);
      }
      const float mb = rbf(mean), rs = __builtin_amdgcn_rcpf(rbf(sqrtf(rbf(rbf(wave_sum(s2) * (1.f / DM)) + 1e-5f))));
#pragma unroll
      for (int s = 0; s < 5; ++s)
        *(LAS unsigned*)(lds + L_X + w * RS_X + 256 * s + 4 * lane) = cvtpk(rbf(bf_lo(xv[s]) - mb) * rs, rbf(bf_hi(xv[s]) - mb) * rs);
      lds_set(lds, 1 + w);
      ST(7);
      lds_wait(lds, 1 + (lane & 3));
      s16x4 af[10];
#pragma unroll
      for (int s = 0; s < 10; ++s) af[s] = *(LAS s16x4*)(lds + L_X + bl * RS_X + (160 * w + 16 * s + 4 * g4) * 2);
      const int rsb = 32 * nt;
      const int a0 = L_W1 + (160 * w + tr_row) * rsb + tr_col;
      wait_vm<0>();
      tag = next_tag(epoch_v);
      ST(9);
#pragma unroll 1
      for (int t = 0; t < nt; ++t) {
        f32x4 d = {0.f, 0.f, 0.f, 0.f};
#pragma unroll
        for (int s = 0; s < 10; s += 2)
          d = mfma32(af[s], af[s + 1], tr_read(lds, a0 + 16 * s * rsb + 32 * t), tr_read(lds, a0 + 16 * (s + 1) * rsb + 32 * t), d);
        *(LAS float*)(lds + L_RED + ((w * 4 + t) * 64 + lane) * 4) = d[0];
      }
      lds_set(lds, 8 + w);
    }
    if (w == 0) {
      lds_wait(lds, 8 + (lane & 3));
#pragma unroll 1
      for (int t = 0; t < nt; ++t) {
        const LAS float* r = (const LAS float*)(lds + L_RED) + t * 64 + lane;
        const float v = (r[0] + r[256]) + (r[512] + r[768]);
        *(LAS u16*)(lds + L_RED + 4096 + (g4 * 16 * nt + 16 * t + i16) * 2) = (u16)cvtpk(v, v);
      }
      const u32x2 pk = *(LAS u32x2*)(lds + L_RED + 4096 + lane * 8);
      const unsigned po = j < 6 ? WS_QP + ((h * 4 + (lane >> 3)) * 96 + 16 * j) * 8 + (lane & 7) * 16
                                : WS_GU + ((((wg & 7) * 2 + (f < 3 ? 0 : 1)) * 4 + g4) * 96 + 32 * (f < 3 ? f : f - 3)) * 8 + i16 * 16;
      if (lane < 16 * nt) __builtin_amdgcn_raw_buffer_store_b128(u32x4{pk[0], tag, pk[1], tag}, rws, po, 0, GST);
      ST(2);
      const unsigned qo = WS_QP + (h * 4 + (j >> 1)) * 768 + min(lane, 47) * 16;
      u32x4 a;
      bool ok = false;
      __builtin_amdgcn_s_sleep(SLQ0);
#pragma unroll 1
      for (unsigned spins = 0; spins < SPIN_CAP; ++spins) {
        asm volatile("" ::: "memory");
        if (!ok) { a = get_granules2(rws, qo); ok = (a[1] == tag) & (a[3] == tag); }
        ++nsweep;
        if (__all(ok)) break;
        __builtin_amdgcn_s_sleep(SLQ);
      }
      if (lane < 48) *(LAS u32x2*)(lds + L_Q + lane * 8) = u32x2{a[0], a[2]};
      lds_set(lds, 16);
      ST(8);
      STV(19, nsweep);
    } else if (w == 1) {
      const int l47 = min(lane, 47), bb = l47 / 12;
      const unsigned go = WS_GU + (((wg & 7) * 8 + bb) * 96 + 24 * (h & 3)) * 8 + (l47 - bb * 12) * 16;
      u32x4 a, b2;
      bool ok = false;
      __builtin_amdgcn_s_sleep(127);
#pragma unroll 1
      for (unsigned spins = 0; spins < SPIN_CAP; ++spins) {
        asm volatile("" ::: "memory");
        if (!ok) {
          a = get_granules2(rws, go); b2 = get_granules2(rws, go + 4 * 768);
          ok = (a[1] == tag) & (a[3] == tag) & (b2[1] == tag) & (b2[3] == tag);
        }
        if (__all(ok)) break;
        __builtin_amdgcn_s_sleep(32);
      }
      float hv[4];
#pragma unroll
      for (int e = 0; e < 4; ++e) {
        const float gg = (e & 1) ? bf_hi(a[e & 2]) : bf_lo(a[e & 2]), uu = (e & 1) ? bf_hi(b2[e & 2]) : bf_lo(b2[e & 2]);
        const float inner = 0.7978845608028654f * (gg + 0.044715f * gg * gg * gg);
        hv[e] = rbf(0.5f * gg * (1.f + tanhf(inner))) * uu;
      }
      if (lane < 48) *(LAS u32x2*)(lds + L_O + bb * RS_O + DH * 2 + (lane - bb * 12) * 8) = u32x2{cvtpk(hv[0], hv[1]), cvtpk(hv[2], hv[3])};
    }
    ST(11);
  }

  bar();
  ST(12);
  const float* mrg = (const float*)(lds + L_MRG);
  if (w < 4) {
    float M = mrg[192];
#pragma unroll
    for (int i = 1; i < 8; ++i) M = fmaxf(M, mrg[i * 200 + 192]);
    const int d = w < 3 ? tid : 193;
    float o = 0.f;
#pragma unroll
    for (int i = 0; i < 8; ++i) o = fmaf(__builtin_amdgcn_exp2f((mrg[i * 200 + 192] - M) * LOG2E), mrg[i * 200 + d], o);
    if (w < 3) put_granule_pairs(rws, p.ws, WS_OP + ((h * 8 + j) * 208 + tid) * 8, o, tag, lane);
    else if (lane == 0) __builtin_amdgcn_raw_buffer_store_b128(u32x4{__float_as_uint(M), tag, __float_as_uint(o), tag}, rws, WS_OP + ((h * 8 + j) * 208 + 192) * 8, 0, AUX_SC1);
  }
  s16x4 wf[15];
  if (w < 5) {
#pragma unroll
    for (int s = 0; s < 15; ++s) wf[s] = tr_read(lds, L_WO + (16 * s + tr_row) * RS_WO + 32 * w + tr_col);
  }
  ST(13);
  if (w < 6) {
    const int bb = tid / 96, pr = tid - bb * 96;
    const unsigned ob = WS_OP + (h * 8 + 2 * bb) * 208 * 8;
    u32x4 a0, a1, m0, m1;
    bool ok = false;
#pragma unroll 1
    for (unsigned spins = 0; spins < SPIN_CAP; ++spins) {
      asm volatile("" ::: "memory");
      if (!ok) {
        a0 = get_granules2(rws, ob + pr * 16);
        a1 = get_granules2(rws, ob + 208 * 8 + pr * 16);
        m0 = get_granules2(rws, ob + 192 * 8);
        m1 = get_granules2(rws, ob + 208 * 8 + 192 * 8);
        ok = (a0[1] == tag) & (a0[3] == tag) & (a1[1] == tag) & (a1[3] == tag) & (m0[1] == tag) & (m0[3] == tag) & (m1[1] == tag) & (m1[3] == tag);
      }
      if (__all(ok)) break;
      __builtin_amdgcn_s_sleep(SL1);
    }
    const float ma = __uint_as_float(m0[0]), mb2 = __uint_as_float(m1[0]), M = fmaxf(ma, mb2);
    const float wa = __builtin_amdgcn_exp2f((ma - M) * LOG2E), wb = __builtin_amdgcn_exp2f((mb2 - M) * LOG2E);
    const float inv = __builtin_amdgcn_rcpf(fmaf(wa, __uint_as_float(m0[2]), wb * __uint_as_float(m1[2])));
    const float ua = wa * inv, ub = wb * inv;
    *(LAS unsigned*)(lds + L_O + bb * RS_O + pr * 4) =
        cvtpk(fmaf(ua, __uint_as_float(a0[0]), ub * __uint_as_float(a1[0])), fmaf(ua, __uint_as_float(a0[2]), ub * __uint_as_float(a1[2])));
  }
  ST(14);
  bar();
  if (w < 5) {
    s16x4 of[15];
#pragma unroll
    for (int s = 0; s < 15; ++s) of[s] = *(LAS s16x4*)(lds + L_O + bl * RS_O + (16 * s + 4 * g4) * 2);
    f32x4 d0 = {0.f, 0.f, 0.f, 0.f}, d1 = d0;
#pragma unroll
    for (int s = 0; s < 8; s += 2) d0 = mfma32(of[s], of[s + 1], wf[s], wf[s + 1], d0);
#pragma unroll
    for (int s = 8; s < 14; s += 2) d1 = mfma32(of[s], of[s + 1], wf[s], wf[s + 1], d1);
    d1 = mfma32(of[14], s16x4{0, 0, 0, 0}, wf[14], wf[14], d1);
    put_granule_pairs(rws, p.ws, WS_YP + (((j * 20 + 4 * w + g4) * 32 + h) * 16 + i16) * 8, d0[0] + d1[0], tag, lane);
    ST(15);
  } else if (w == 6 && h < 20) {
    const int oi = (h & 3) * DM + 80 * j + 16 * (h >> 2) + 2 * (lane & 7);
    const unsigned xr = *(const unsigned*)(p.x + oi);
    const unsigned yb = WS_YP + (j * 20 + h) * 512 * 8 + lane * 16;
    u32x4 a[4];
    bool ok[4] = {false, false, false, false};
#pragma unroll 1
    for (unsigned spins = 0; spins < SPIN_CAP; ++spins) {
      asm volatile("" ::: "memory");
#pragma unroll
      for (int i = 0; i < 4; ++i)
        if (!ok[i]) a[i] = get_granules2(rws, yb + i * 1024);
#pragma unroll
      for (int i = 0; i < 4; ++i) ok[i] = (a[i][1] == tag) & (a[i][3] == tag);
      if (__all(ok[0] & ok[1] & ok[2] & ok[3])) break;
      __builtin_amdgcn_s_sleep(SL2);
    }
    float s0 = (__uint_as_float(a[0][0]) + __uint_as_float(a[1][0])) + (__uint_as_float(a[2][0]) + __uint_as_float(a[3][0]));
    float s1 = (__uint_as_float(a[0][2]) + __uint_as_float(a[1][2])) + (__uint_as_float(a[2][2]) + __uint_as_float(a[3][2]));
    s0 += dpp<XOR8>(s0); s1 += dpp<XOR8>(s1);
    s0 = x16_sum(s0); s1 = x16_sum(s1);
    if (lane < 8) *(unsigned*)(p.out + oi) = cvtpk(bf_lo(xr) + s0, bf_hi(xr) + s1);
    if (wg == 0 && lane == 0) __hip_atomic_store((GAS unsigned*)p.ws, tag, __ATOMIC_RELAXED, __HIP_MEMORY_SCOPE_AGENT);
    ST(16);
  }
  ST_END();
}

extern "C" void kernel_launch(void* const* d_in, const int* in_sizes, int n_in, void* d_out, int out_size, void* d_ws,
                              size_t ws_size, hipStream_t stream) {
  Params p;
  p.x = (const u16*)d_in[0];
  p.kc = (const u16*)d_in[1];
  p.vc = (const u16*)d_in[2];
  p.wq = (const u16*)d_in[3];
  p.wo = (const u16*)d_in[4];
  p.wg = (const u16*)d_in[6];
  p.wu = (const u16*)d_in[7];
  p.wd = (const u16*)d_in[8];
  p.out = (u16*)d_out;
  p.ws = (unsigned char*)d_ws + (WSPOS == 0 ? 0 : WSPOS == 1 ? ((ws_size - WS_END) & ~(size_t)4095) : ((ws_size - WS_END - ((size_t)WSPOS << 20)) & ~(size_t)4095));
  fused_decode<<<256, NTHR, 0, stream>>>(p);
}
```

```cpp
#include <hip/hip_runtime.h>
#include <hip/hip_bf16.h>
#include <stdint.h>

#define RING 8
#define FLY 5
#define LEAD 2
#define CATCH 4
#define WQW 4
#define CODELEN 16000
#define SL1 2
#define SL2 2
#define SLQ 16
#define SLQ0 64
#define TS 0
#define TA 315
#define L1A 16
#define L2A 16
#define S1A 16
#define S2A 16
#define XP 0
#define ROTM 0
#define SWAP 1
#define SLQN 1
#define QPOLL 15
#define LPOLL 24
#define PRIO 0
#define STAMP 0

typedef unsigned short u16;
typedef unsigned u32x2 __attribute__((ext_vector_type(2)));
typedef unsigned u32x4 __attribute__((ext_vector_type(4)));
typedef float f32x2 __attribute__((ext_vector_type(2)));
typedef float f32x4 __attribute__((ext_vector_type(4)));
typedef short s16x4 __attribute__((ext_vector_type(4)));
typedef short s16x8 __attribute__((ext_vector_type(8)));
typedef __bf16 bf16x2 __attribute__((ext_vector_type(2)));
typedef __bf16 bf16x8 __attribute__((ext_vector_type(8)));
#define LAS __attribute__((address_space(3)))
#define GAS __attribute__((address_space(1)))
typedef __amdgpu_buffer_rsrc_t rsrc_t;

constexpr int DM = 640, NH = 32, DH = 192, SK = 8192, WIN = 2048, FF = 1536, QN = NH * DH;
constexpr int NTHR = 832;
constexpr float LOG2E = 1.4426950408889634f;
constexpr unsigned SPIN_CAP = 1u << 18;

constexpr unsigned WS_QP = 4096;
constexpr unsigned WS_GU = WS_QP + 32 * 4 * 96 * 8;
constexpr unsigned WS_ML = WS_GU + 8 * 2 * 4 * 96 * 8;
constexpr unsigned WS_OP = WS_ML + 32 * 8 * 128;
constexpr unsigned WS_YP = WS_OP + 32 * 8 * 96 * 8;
constexpr unsigned WS_END = WS_YP + 8 * 10 * 32 * 128;

constexpr int RS_WO = 160, RS_O = 496, RS_X = 1296;
constexpr int L_W1 = 0;
constexpr int L_WO = L_W1 + 640 * 128;
constexpr int L_MRG = L_WO + 240 * RS_WO;
constexpr int L_Q = L_MRG + 8 * 768;
constexpr int L_O = L_Q + 384;
constexpr int L_X = L_O + 4 * RS_O;
constexpr int L_RED = L_X + 4 * RS_X;
constexpr int L_ML = L_RED + 4096 + 512;
constexpr int L_FLAG = L_ML + 128;
constexpr int L_END = L_FLAG + 256;
constexpr int F_LN = 0, F_RED = 4, F_Q = 8, F_SCALE = 9, F_WO = 16, F_ML = 24, F_P1 = 32, F_P2 = 40;

__device__ __forceinline__ float bf_lo(unsigned u) { return __uint_as_float(u << 16); }
__device__ __forceinline__ float bf_hi(unsigned u) { return __uint_as_float(u & 0xffff0000u); }
__device__ __forceinline__ unsigned cvtpk(float lo, float hi) {
  bf16x2 r = {(__bf16)lo, (__bf16)hi};
  return __builtin_bit_cast(unsigned, r);
}
__device__ __forceinline__ float rbf(float x) { return bf_lo(cvtpk(x, 0.f)); }
__device__ __forceinline__ float dot2(unsigned k, unsigned q, float acc) {
  return __builtin_amdgcn_fdot2_f32_bf16(__builtin_bit_cast(bf16x2, k), __builtin_bit_cast(bf16x2, q), acc, false);
}
template <int CTRL> __device__ __forceinline__ unsigned dppu(unsigned x) { return (unsigned)__builtin_amdgcn_mov_dpp((int)x, CTRL, 0xf, 0xf, true); }
template <int CTRL> __device__ __forceinline__ float dpp(float x) { return __uint_as_float(dppu<CTRL>(__float_as_uint(x))); }
constexpr int XOR1 = 0xB1, XOR2 = 0x4E, XOR7 = 0x141, XOR8 = 0x128;
__device__ __forceinline__ float swap16_add(float a, float b) {
  auto s = __builtin_amdgcn_permlane16_swap(__float_as_uint(a), __float_as_uint(b), false, false);
  return __uint_as_float(s[0]) + __uint_as_float(s[1]);
}
__device__ __forceinline__ float swap32_add(float a, float b) {
  auto s = __builtin_amdgcn_permlane32_swap(__float_as_uint(a), __float_as_uint(b), false, false);
  return __uint_as_float(s[0]) + __uint_as_float(s[1]);
}
__device__ __forceinline__ float x16_sum(float x) { x = swap16_add(x, x); return swap32_add(x, x); }
__device__ __forceinline__ float x16_max(float x) {
  auto s = __builtin_amdgcn_permlane16_swap(__float_as_uint(x), __float_as_uint(x), false, false);
  x = fmaxf(__uint_as_float(s[0]), __uint_as_float(s[1]));
  auto t = __builtin_amdgcn_permlane32_swap(__float_as_uint(x), __float_as_uint(x), false, false);
  return fmaxf(__uint_as_float(t[0]), __uint_as_float(t[1]));
}
__device__ __forceinline__ float wave_sum(float x) {
  x += dpp<XOR1>(x); x += dpp<XOR2>(x); x += dpp<XOR7>(x); x += dpp<XOR8>(x);
  return x16_sum(x);
}
__device__ __forceinline__ void bar() { asm volatile("s_waitcnt lgkmcnt(0)\n\ts_barrier" ::: "memory"); }
template <int N> __device__ __forceinline__ void wait_vm() { asm volatile("s_waitcnt vmcnt(%0)" ::"n"(N) : "memory"); }
__device__ __forceinline__ rsrc_t mk_rsrc(const void* p, unsigned bytes) {
  return __builtin_amdgcn_make_buffer_rsrc((void*)p, 0, bytes, 0x00020000);
}
constexpr int AUX_NT = 2, AUX_SC1 = 16;
template <int AUX = AUX_SC1> __device__ __forceinline__ void put2(rsrc_t rs, unsigned off, unsigned a, unsigned b, unsigned tag) {
  __builtin_amdgcn_raw_buffer_store_b128(u32x4{a, tag, b, tag}, rs, off, 0, AUX);
}
template <int AUX> __device__ __forceinline__ u32x4 get2(rsrc_t rs, unsigned off) { return __builtin_amdgcn_raw_buffer_load_b128(rs, off, 0, AUX); }
__device__ __forceinline__ bool fresh(u32x4 a, unsigned tag) { return (a[1] == tag) & (a[3] == tag); }
template <int N, int SLEEP, int AUX = AUX_SC1> __device__ __forceinline__ void gather(rsrc_t rs, unsigned off, unsigned S, unsigned tag, u32x4 (&a)[N]) {
  bool ok = false;
#pragma unroll 1
  for (unsigned spins = 0; spins < SPIN_CAP; ++spins) {
    asm volatile("" ::: "memory");
    if (!ok) {
#pragma unroll
      for (int i = 0; i < N; ++i) a[i] = get2<AUX>(rs, off + i * S);
      ok = true;
#pragma unroll
      for (int i = 0; i < N; ++i) ok &= fresh(a[i], tag);
    }
    if (__all(ok)) break;
    __builtin_amdgcn_s_sleep(SLEEP);
  }
}
__device__ __forceinline__ s16x4 tr_read(unsigned char* lds, int off) {
  return __builtin_amdgcn_ds_read_tr16_b64_v4i16((LAS s16x4*)(lds + off));
}
__device__ __forceinline__ f32x4 mfma32(s16x4 a0, s16x4 a1, s16x4 b0, s16x4 b1, f32x4 c) {
  const s16x8 a = __builtin_shufflevector(a0, a1, 0, 1, 2, 3, 4, 5, 6, 7), b = __builtin_shufflevector(b0, b1, 0, 1, 2, 3, 4, 5, 6, 7);
  return __builtin_amdgcn_mfma_f32_16x16x32_bf16(__builtin_bit_cast(bf16x8, a), __builtin_bit_cast(bf16x8, b), c, 0, 0, 0);
}
__device__ __forceinline__ unsigned lds_addr(unsigned char* lds, int off) { return (unsigned)(size_t)(LAS unsigned char*)lds + off; }
__device__ __forceinline__ void lds_set(unsigned char* lds, int flag) {
  asm volatile("s_waitcnt lgkmcnt(0)\n\tds_write_b32 %0, %1" ::"v"(lds_addr(lds, L_FLAG + 4 * flag)), "v"(1u) : "memory");
}
__device__ __forceinline__ void lds_wait(unsigned char* lds, int flag) {
  unsigned t, n = SPIN_CAP << 4;
  asm volatile("1:\n\tds_read_b32 %0, %2\n\ts_waitcnt lgkmcnt(0)\n\tv_cmp_eq_u32 vcc, 0, %0\n\ts_cbranch_vccz 2f\n\t"
               "s_sleep 1\n\ts_sub_u32 %1, %1, 1\n\ts_cbranch_scc0 1b\n2:"
               : "=&v"(t), "+s"(n) : "v"(lds_addr(lds, L_FLAG + 4 * flag)) : "vcc", "scc", "memory");
}
__device__ __forceinline__ void dma16(const void* base, unsigned ldsaddr, int voff) {
  const unsigned long long b = (unsigned long long)base;
  const u32x4 rs = {(unsigned)b, (unsigned)(b >> 32) & 0xffffu, 0x7fffffffu, 0x00020000u};
  unsigned keep;
  asm volatile("s_mov_b32 %0, m0\n\ts_mov_b32 m0, %1\n\ts_nop 4\n\tbuffer_load_dwordx4 %2, %3, 0 offen nt lds\n\ts_mov_b32 m0, %0"
               : "=&s"(keep) : "s"(ldsaddr), "v"(voff), "s"(rs) : "memory");
}
__device__ __forceinline__ void wait_until(unsigned t) {
  asm volatile("1:\n\ts_memrealtime vcc\n\ts_waitcnt lgkmcnt(0)\n\ts_sub_u32 vcc_lo, vcc_lo, %0\n\ts_cmp_lt_i32 vcc_lo, 0\n\t"
               "s_cbranch_scc0 2f\n\ts_sleep 1\n\ts_branch 1b\n2:" ::"s"(t) : "vcc", "scc");
}
__device__ __forceinline__ unsigned next_tag(unsigned epoch_v) { return __builtin_amdgcn_readfirstlane(epoch_v) + 1u; }
template <int I> struct ic { static constexpr int value = I; };
template <int I, int N, class F> __device__ __forceinline__ void static_for(F&& f) {
  if constexpr (I < N) { f(ic<I>{}); static_for<I + 1, N>(f); }
}
__host__ __device__ constexpr int issued(int G) {
  int a = G < 0 ? RING : RING + (G >= 1 ? (G + CATCH - 1) / CATCH : 0), b = G + 1 + LEAD, n = a > b ? a : b;
  return n > 32 ? 32 : n;
}

#define ST(i) do {} while (0)
#define ST_END() do {} while (0)
#define PST(i) do {} while (0)

struct Params {
  const u16 *x, *kc, *vc, *wq, *wo, *wg, *wu, *wd;
  u16* out;
  unsigned char* ws;
};

__global__ void __launch_bounds__(NTHR, 4) fused_decode(Params p) {
  __shared__ __attribute__((aligned(16))) unsigned char lds[L_END];
  const int tid = threadIdx.x, lane = tid & 63, w = __builtin_amdgcn_readfirstlane(tid >> 6);
  const int wg = blockIdx.x, idx = wg >> 3, h = (wg & 7) * 4 + (idx >> 3), j = idx & 7;
  const int g4 = lane >> 4, i16 = lane & 15, bl = (lane >> 2) & 3;
  const int tr_row = 4 * g4 + bl, tr_col = 8 * (lane & 3);
  const rsrc_t rws = mk_rsrc(p.ws, WS_END);
  const unsigned epoch_v = __hip_atomic_load((GAS unsigned*)p.ws, __ATOMIC_RELAXED, __HIP_MEMORY_SCOPE_AGENT);
  unsigned tag;
  unsigned stampv = 0;

  ST(0);
  const unsigned tstart = TS ? (unsigned)__builtin_amdgcn_s_memrealtime() : 0u;
  const int f = (idx >> 3) * 2 + j - 6, nt = j < 6 ? 2 : f < 6 ? 4 : 0;
  unsigned xv[5];
  if (w >= 4) {
    const u16* bo = p.wo + (size_t)DH * h * DM + 80 * j;
    const u16* bd = p.wd + (size_t)48 * h * DM + 80 * j - 1920 / 10 * DM;
    if (w == 4) *((LAS unsigned*)(lds + L_FLAG) + lane) = 0u;
    bar();
    ST(20);
#pragma unroll 1
    for (int pc = w > 4 ? w - 5 : 38; pc < 38; pc += 8) {
      const int ci = pc * 64 + lane, row = (ci * 6554) >> 16;
      if (XP & 1) { if (pc < 37 || lane < 32) dma16(pc < 30 ? p.wo + (size_t)(h * 8 + j) * 15360 : p.wd + (size_t)(h * 8 + j) * 3840 - 15360, lds_addr(lds, L_WO) + pc * 1024, ci * 16); } else
      if (pc < 37 || lane < 32) dma16(pc < 30 ? bo : bd, lds_addr(lds, L_WO) + pc * 1024, row * (DM * 2 - 160) + ci * 16);
    }
  } else {
    const u16* xr = p.x + w * DM;
    asm volatile("global_load_dword %0, %5, %6\n\tglobal_load_dword %1, %5, %6 offset:256\n\tglobal_load_dword %2, %5, %6 offset:512\n\t"
                 "global_load_dword %3, %5, %6 offset:768\n\tglobal_load_dword %4, %5, %6 offset:1024"
                 : "=&v"(xv[0]), "=&v"(xv[1]), "=&v"(xv[2]), "=&v"(xv[3]), "=&v"(xv[4]) : "v"(lane * 4), "s"(xr) : "memory");
    const u16* bw = j < 6 ? p.wq + (size_t)160 * w * QN + h * DH + 32 * j
                          : (f < 3 ? p.wg : p.wu) + (size_t)160 * w * FF + 192 * (wg & 7) + 64 * (f < 3 ? f : f - 3);
    const int sh = j < 6 ? 2 : 3; int gs = (j < 6 ? QN * 2 : FF * 2) << (6 - sh);
    int vo = (lane >> sh) * (j < 6 ? QN * 2 : FF * 2) + (lane & ((1 << sh) - 1)) * 16;
    if ((XP & 2) && j < 6) {
      bw = p.wq + (size_t)(320 * (j & 1) + 80 * w) * QN + h * DH + 64 * (j >> 1);
      vo = (lane >> 3) * QN * 2 + (lane & 7) * 16; gs = QN * 2 * 8;
    }
#pragma unroll 1
    for (int i = 0; i < 5 * nt; ++i, vo += gs) dma16(bw, lds_addr(lds, L_W1) + (w * 5 * nt + i) * 1024, vo);
    bar();
  }
  int wr = w;
  asm volatile("" : "+s"(wr));
  s16x4 wf[15];

  if (wr > 4) {
    ST(18);
    const int sw = w - 5, r = lane >> 3, c = lane & 7;
    const size_t kvoff = ((size_t)((j >> 1) * NH + h) * SK + (SK - WIN) + (j & 1) * 1024) * DH;
    const rsrc_t rk = mk_rsrc(p.kc + kvoff, 1024 * DH * 2), rv = mk_rsrc(p.vc + kvoff, 1024 * DH * 2);
    const int voff = r * 384 + c * 16;
    u32x4 ring[RING][3];
    const unsigned hs = ((unsigned)(ROTM == 3 ? wg : wg * 8 + sw) * 2654435761u) >> 16;
    const int rot = ROTM == 0 ? idx : hs >> 3, rmul = ROTM == 2 || ROTM == 3 ? (hs & 7) * 2 + 1 : 1;
#define LOAD(G, jj) __builtin_amdgcn_raw_buffer_load_b128((G) < 16 ? rk : rv, voff + (jj) * 128, ((((G) * rmul + rot) & 15) * 8 + sw) * 3072, AUX_NT)
    __builtin_amdgcn_sched_barrier(0);
    if (PRIO && sw >= 4) __builtin_amdgcn_s_setprio(1);
    wait_vm<WQW>();
    ST(1);
    static_for<0, 3 * RING>([&](auto nc) {
      constexpr int N = decltype(nc)::value;
      if constexpr (N >= FLY) wait_vm<FLY - 1>();
      if constexpr (N >= FLY && (N - FLY) % 3 == 2) PST((N - FLY) / 3);
      if constexpr (N == FLY) {
        lds_set(lds, F_WO + sw);
        ST(21);
        ST(22);
      }
      if constexpr (N == QPOLL) lds_set(lds, F_P1 + sw);
      ring[N / 3][N % 3] = LOAD(N / 3, N % 3);
      __builtin_amdgcn_sched_barrier(0);
    });
    ST(3);
    lds_wait(lds, F_Q);
    ST(4);
    tag = next_tag(epoch_v);
    asm volatile("" : "+s"(tag));
    u32x4 qv[3];
#pragma unroll
    for (int jj = 0; jj < 3; ++jj) qv[jj] = *(LAS u32x4*)(lds + L_Q + jj * 128 + c * 16);
#define STEP(G)                                                                                         \
  do {                                                                                                  \
    __builtin_amdgcn_sched_barrier(0);                                                                  \
    if constexpr (PRIO && (G) == PRIO) __builtin_amdgcn_s_setprio(0);                                   \
    if constexpr (STAMP == 2 && (G) >= (3 * RING - FLY) / 3) { wait_vm<3 * (issued((G) - 1) - (G) - 1)>(); PST(G); } \
    if constexpr (issued(G) > issued((G) - 1)) {                                                        \
      wait_vm<3 * (issued((G) - 1) - (G) - 1)>();                                                       \
      if (TS) wait_until(tstart + TA + issued((G) - 1) * TS);                                           \
      _Pragma("unroll") for (int N = 3 * issued((G) - 1); N < 3 * issued(G); ++N) ring[(N / 3) % RING][N % 3] = LOAD(N / 3, N % 3); \
    }                                                                                                   \
    __builtin_amdgcn_sched_barrier(0);                                                                  \
  } while (0)
    float sc[16];
    static_for<0, 16>([&](auto gc) {
      constexpr int G = decltype(gc)::value;
      STEP(G);
      float d0 = 0.f, d1 = 0.f, d2 = 0.f;
#pragma unroll
      for (int k = 0; k < 4; ++k) {
        d0 = dot2(ring[G % RING][0][k], qv[0][k], d0);
        d1 = dot2(ring[G % RING][1][k], qv[1][k], d1);
        d2 = dot2(ring[G % RING][2][k], qv[2][k], d2);
      }
      float d = (d0 + d1) + d2;
      d += dpp<XOR1>(d); d += dpp<XOR2>(d); d += dpp<XOR7>(d);
      sc[G] = rbf(rbf(d) * 0.07216878364870322f);
      asm volatile("" : "+v"(sc[G]));
    });
    ST(5);
    float m = sc[0];
#pragma unroll
    for (int G = 1; G < 16; ++G) m = fmaxf(m, sc[G]);
    m = fmaxf(m, dpp<XOR8>(m));
    m = x16_max(m);
    float l = 0.f;
#pragma unroll
    for (int G = 0; G < 16; ++G) { sc[G] = __builtin_amdgcn_exp2f((sc[G] - m) * LOG2E); l += sc[G]; }
    l += dpp<XOR8>(l);
    l = x16_sum(l);
    *(LAS f32x2*)(lds + L_ML + sw * 8) = f32x2{m, l};
    lds_set(lds, F_ML + sw);
    f32x2 acc[12];
    float scale;
#pragma unroll
    for (int k = 0; k < 12; ++k) acc[k] = f32x2{0.f, 0.f};
    static_for<16, 32>([&](auto gc) {
      constexpr int G = decltype(gc)::value;
      STEP(G);
      if constexpr (G == LPOLL) lds_set(lds, F_P2 + sw);
      if constexpr (G == 30) {
        lds_wait(lds, F_SCALE);
        const f32x2 ml = *(LAS f32x2*)(lds + L_ML + 64);
        scale = __builtin_amdgcn_exp2f((m - ml[0]) * LOG2E) * ml[1];
      }
      const f32x2 pp = {sc[G - 16], sc[G - 16]};
#pragma unroll
      for (int jj = 0; jj < 3; ++jj)
#pragma unroll
        for (int k = 0; k < 4; ++k) {
          const unsigned u = ring[G % RING][jj][k];
          acc[jj * 4 + k] = __builtin_elementwise_fma(f32x2{bf_lo(u), bf_hi(u)}, pp, acc[jj * 4 + k]);
        }
    });
#undef STEP
#undef LOAD
    ST(6);
    ST(17);
    float t12[12], t6[6];
#pragma unroll
    for (int k = 0; k < 12; ++k) t12[k] = swap32_add(acc[k >> 1][k & 1], acc[(k + 12) >> 1][k & 1]);
#pragma unroll
    for (int k = 0; k < 6; ++k) t6[k] = swap16_add(t12[k], t12[k + 6]);
    LAS float* mrg = (LAS float*)(lds + L_MRG) + sw * DH;
#pragma unroll
    for (int k = 0; k < 3; ++k) {
      const float a = t6[k] + dpp<XOR8>(t6[k]), b2 = t6[k + 3] + dpp<XOR8>(t6[k + 3]);
      const int e = 3 * r + k;
      mrg[(e >> 3) * 64 + c * 8 + (e & 7)] = ((lane & 8) ? b2 : a) * scale;
    }
  } else {
    __builtin_amdgcn_s_setprio(3);
    if (w == 4) {
      tag = next_tag(epoch_v);
      const int l47 = min(lane, 47), bb = l47 / 12;
      {
        u32x4 a[1];
        lds_wait(lds, F_P1);
        gather<1, SLQ>(rws, WS_QP + (h * 4 + (j >> 1)) * 768 + l47 * 16, 0, tag, a);
        *(LAS u32x2*)(lds + L_Q + l47 * 8) = u32x2{a[0][0], a[0][2]};
        lds_set(lds, F_Q);
        ST(8);
      }
      lds_wait(lds, F_ML + (lane & 7));
      const f32x2 ml = *(LAS f32x2*)(lds + L_ML + (lane & 7) * 8);
      float M = fmaxf(ml[0], dpp<XOR1>(ml[0]));
      M = fmaxf(M, dpp<XOR2>(M)); M = fmaxf(M, dpp<XOR7>(M));
      float L = __builtin_amdgcn_exp2f((ml[0] - M) * LOG2E) * ml[1];
      L += dpp<XOR1>(L); L += dpp<XOR2>(L); L += dpp<XOR7>(L);
      if (lane == 0) put2(rws, WS_ML + (h * 8 + j) * 128, __float_as_uint(M), __float_as_uint(L), tag);
      {
        u32x4 a[2];
        gather<2, 32>(rws, WS_GU + (((wg & 7) * 8 + bb) * 96 + 24 * (h & 3)) * 8 + (l47 - bb * 12) * 16, 4 * 768, tag, a);
        float hv[4];
#pragma unroll
        for (int e = 0; e < 4; ++e) {
          const float gg = (e & 1) ? bf_hi(a[0][e & 2]) : bf_lo(a[0][e & 2]), uu = (e & 1) ? bf_hi(a[1][e & 2]) : bf_lo(a[1][e & 2]);
          const float inner = 0.7978845608028654f * (gg + 0.044715f * gg * gg * gg);
          hv[e] = rbf(0.5f * gg * (1.f + tanhf(inner))) * uu;
        }
        *(LAS u32x2*)(lds + L_O + bb * RS_O + DH * 2 + (l47 - bb * 12) * 8) = u32x2{cvtpk(hv[0], hv[1]), cvtpk(hv[2], hv[3])};
        ST(10);
      }
      {
        u32x4 a[1];
        lds_wait(lds, F_P2);
        gather<1, 16>(rws, WS_ML + (h * 8 + (j ^ 1)) * 128, 0, tag, a);
        const float M2 = __uint_as_float(a[0][0]), MM = fmaxf(M, M2);
        const float e1 = __builtin_amdgcn_exp2f((M - MM) * LOG2E) * L, e2 = __builtin_amdgcn_exp2f((M2 - MM) * LOG2E) * __uint_as_float(a[0][2]);
        *(LAS f32x2*)(lds + L_ML + 64) = f32x2{MM, __builtin_amdgcn_rcpf(e1 + e2)};
        lds_set(lds, F_SCALE);
        ST(11);
      }
    } else {
      if (nt == 0) wait_vm<0>();
      asm volatile("s_waitcnt vmcnt(10)" : "+v"(xv[0]), "+v"(xv[1]), "+v"(xv[2]), "+v"(xv[3]), "+v"(xv[4]));
      float s1 = 0.f;
#pragma unroll
      for (int s = 0; s < 5; ++s) s1 += bf_lo(xv[s]) + bf_hi(xv[s]);
      const float mean = wave_sum(s1) * (1.f / DM);
      float s2 = 0.f;
#pragma unroll
      for (int s = 0; s < 5; ++s) {
        const float a = bf_lo(xv[s]) - mean, c = bf_hi(xv[s]) - mean;
        s2 = fmaf(a, a, s2); s2 = fmaf(c, c, s2);
      }
      const float mb = rbf(mean), rs = __builtin_amdgcn_rcpf(rbf(sqrtf(rbf(rbf(wave_sum(s2) * (1.f / DM)) + 1e-5f))));
#pragma unroll
      for (int s = 0; s < 5; ++s)
        *(LAS unsigned*)(lds + L_X + w * RS_X + 256 * s + 4 * lane) = cvtpk(rbf(bf_lo(xv[s]) - mb) * rs, rbf(bf_hi(xv[s]) - mb) * rs);
      lds_set(lds, F_LN + w);
      ST(7);
      lds_wait(lds, F_LN + (lane & 3));
      s16x4 af[10];
#pragma unroll
      for (int s = 0; s < 10; ++s) af[s] = *(LAS s16x4*)(lds + L_X + bl * RS_X + (160 * w + 16 * s + 4 * g4) * 2);
      const int rsb = 32 * nt, a0 = L_W1 + (160 * w + tr_row) * rsb + tr_col;
      wait_vm<0>();
      tag = next_tag(epoch_v);
      ST(9);
#pragma unroll 1
      for (int t = 0; t < nt; ++t) {
        f32x4 d = {0.f, 0.f, 0.f, 0.f};
#pragma unroll
        for (int s = 0; s < 10; s += 2)
          d = mfma32(af[s], af[s + 1], tr_read(lds, a0 + 16 * s * rsb + 32 * t), tr_read(lds, a0 + 16 * (s + 1) * rsb + 32 * t), d);
        *(LAS float*)(lds + L_RED + ((w * 4 + t) * 64 + lane) * 4) = d[0];
      }
      lds_set(lds, F_RED + w);
      if (w == 0) {
        lds_wait(lds, F_RED + (lane & 3));
#pragma unroll 1
        for (int t = 0; t < nt; ++t) {
          const LAS float* r = (const LAS float*)(lds + L_RED) + t * 64 + lane;
          const float v = (r[0] + r[256]) + (r[512] + r[768]);
          *(LAS u16*)(lds + L_RED + 4096 + (g4 * 16 * nt + 16 * t + i16) * 2) = (u16)cvtpk(v, v);
        }
        const u32x2 pk = *(LAS u32x2*)(lds + L_RED + 4096 + lane * 8);
        const unsigned po = j < 6 ? WS_QP + ((h * 4 + (lane >> 3)) * 96 + 16 * j) * 8 + (lane & 7) * 16
                                  : WS_GU + ((((wg & 7) * 2 + (f < 3 ? 0 : 1)) * 4 + g4) * 96 + 32 * (f < 3 ? f : f - 3)) * 8 + i16 * 16;
        if (lane < 16 * nt) put2(rws, po, pk[0], pk[1], tag);
        ST(2);
      }
    }
    lds_wait(lds, F_WO + (lane & 7));
#pragma unroll
    for (int s = 0; s < 15; ++s) wf[s] = tr_read(lds, L_WO + (16 * s + tr_row) * RS_WO + 32 * w + tr_col);
  }

  bar();
  ST(12);
  if (w == 5) {
    const int l47 = min(lane, 47);
    f32x4 o = *(LAS f32x4*)(lds + L_MRG + l47 * 16);
#pragma unroll
    for (int i = 1; i < 8; ++i) o += *(LAS f32x4*)(lds + L_MRG + i * 768 + l47 * 16);
    if (lane < 48) put2<S1A>(rws, WS_OP + (h * 8 + j) * 768 + lane * 16, cvtpk(o[0], o[1]), cvtpk(o[2], o[3]), tag);
    ST(13);
  } else if (w >= 6 && w < 9) {
    const int t = tid - 6 * 64, bb = t / 48, q4 = t - bb * 48;
    u32x4 a[2];
    gather<2, SL1, L1A>(rws, WS_OP + (h * 8 + 2 * bb) * 768 + q4 * 16, 768, tag, a);
    *(LAS u32x2*)(lds + L_O + bb * RS_O + q4 * 8) = u32x2{cvtpk(bf_lo(a[0][0]) + bf_lo(a[1][0]), bf_hi(a[0][0]) + bf_hi(a[1][0])),
                                                          cvtpk(bf_lo(a[0][2]) + bf_lo(a[1][2]), bf_hi(a[0][2]) + bf_hi(a[1][2]))};
    ST(14);
  }
  bar();
  if (w < 5) {
    s16x4 of[15];
#pragma unroll
    for (int s = 0; s < 15; ++s) of[s] = *(LAS s16x4*)(lds + L_O + bl * RS_O + (16 * s + 4 * g4) * 2);
    f32x4 d0 = {0.f, 0.f, 0.f, 0.f}, d1 = d0;
#pragma unroll
    for (int s = 0; s < 8; s += 2) d0 = mfma32(of[s], of[s + 1], wf[s], wf[s + 1], d0);
#pragma unroll
    for (int s = 8; s < 14; s += 2) d1 = mfma32(of[s], of[s + 1], wf[s], wf[s + 1], d1);
    d1 = mfma32(of[14], s16x4{0, 0, 0, 0}, wf[14], wf[14], d1);
    const float y = d0[0] + d1[0];
    const unsigned pk = cvtpk(y, dpp<XOR1>(y)), pk2 = dppu<XOR2>(pk);
    if (!(lane & 3)) put2<S2A>(rws, WS_YP + ((j * 10 + 2 * w + (g4 >> 1)) * 32 + h) * 128 + (g4 & 1) * 64 + i16 * 4, pk, pk2, tag);
    ST(15);
  } else if (w == 9 && h < 10) {
    const int oi = (2 * (h & 1) + ((lane >> 2) & 1)) * DM + 80 * j + 16 * (h >> 1) + 4 * (lane & 3);
    const u32x2 xr = *(const u32x2*)(p.x + oi);
    u32x4 a[4];
    gather<4, SL2, L2A>(rws, WS_YP + (j * 10 + h) * 4096 + lane * 16, 1024, tag, a);
    float s[4];
#pragma unroll
    for (int e = 0; e < 4; ++e) {
      float v[4];
#pragma unroll
      for (int i = 0; i < 4; ++i) v[i] = (e & 1) ? bf_hi(a[i][e & 2]) : bf_lo(a[i][e & 2]);
      s[e] = (v[0] + v[1]) + (v[2] + v[3]);
      s[e] += dpp<XOR8>(s[e]);
      s[e] = x16_sum(s[e]);
    }
    if (lane < 8) *(u32x2*)(p.out + oi) = u32x2{cvtpk(bf_lo(xr[0]) + s[0], bf_hi(xr[0]) + s[1]), cvtpk(bf_lo(xr[1]) + s[2], bf_hi(xr[1]) + s[3])};
    if (wg == 0 && lane == 0) __hip_atomic_store((GAS unsigned*)p.ws, tag, __ATOMIC_RELAXED, __HIP_MEMORY_SCOPE_AGENT);
    ST(16);
  }
  ST_END();
}

extern "C" void kernel_launch(void* const* d_in, const int* in_sizes, int n_in, void* d_out, int out_size, void* d_ws,
                              size_t ws_size, hipStream_t stream) {
  Params p;
  p.x = (const u16*)d_in[0];
  p.kc = (const u16*)d_in[1];
  p.vc = (const u16*)d_in[2];
  p.wq = (const u16*)d_in[3];
  p.wo = (const u16*)d_in[4];
  p.wg = (const u16*)d_in[6];
  p.wu = (const u16*)d_in[7];
  p.wd = (const u16*)d_in[8];
  p.out = (u16*)d_out;
  p.ws = (unsigned char*)d_ws;
  fused_decode<<<256, NTHR, 0, stream>>>(p);
}
```

```cpp
#include <hip/hip_runtime.h>
#include <hip/hip_bf16.h>
#include <stdint.h>

#define RING 8
#define FLY 5
#define LEAD 2
#define CATCH 4
#define WQW 4
#define CODELEN 16000
#define SL1 2
#define SL2 2
#define SLQ 16
#define SLQ0 64
#define TS 0
#define TA 315
#define L1A 16
#define L2A 16
#define S1A 16
#define S2A 16
#define XP 0
#define ROTM 0
#define SWAP 1
#define SLQN 1
#define QPOLL 15
#define LPOLL 24
#define PRIO 0
#define KAUX 2
#define VAUX 2
#define STAMP 0

typedef unsigned short u16;
typedef unsigned u32x2 __attribute__((ext_vector_type(2)));
typedef unsigned u32x4 __attribute__((ext_vector_type(4)));
typedef float f32x2 __attribute__((ext_vector_type(2)));
typedef float f32x4 __attribute__((ext_vector_type(4)));
typedef short s16x4 __attribute__((ext_vector_type(4)));
typedef short s16x8 __attribute__((ext_vector_type(8)));
typedef __bf16 bf16x2 __attribute__((ext_vector_type(2)));
typedef __bf16 bf16x8 __attribute__((ext_vector_type(8)));
#define LAS __attribute__((address_space(3)))
#define GAS __attribute__((address_space(1)))
typedef __amdgpu_buffer_rsrc_t rsrc_t;

constexpr int DM = 640, NH = 32, DH = 192, SK = 8192, WIN = 2048, FF = 1536, QN = NH * DH;
constexpr int NTHR = 832;
constexpr float LOG2E = 1.4426950408889634f;
constexpr unsigned SPIN_CAP = 1u << 18;

constexpr unsigned WS_QP = 4096;
constexpr unsigned WS_GU = WS_QP + 32 * 4 * 96 * 8;
constexpr unsigned WS_ML = WS_GU + 8 * 2 * 4 * 96 * 8;
constexpr unsigned WS_OP = WS_ML + 32 * 8 * 128;
constexpr unsigned WS_YP = WS_OP + 32 * 8 * 96 * 8;
constexpr unsigned WS_END = WS_YP + 8 * 10 * 32 * 128;

constexpr int RS_WO = 160, RS_O = 496, RS_X = 1296;
constexpr int L_W1 = 0;
constexpr int L_WO = L_W1 + 640 * 128;
constexpr int L_MRG = L_WO + 240 * RS_WO;
constexpr int L_Q = L_MRG + 8 * 768;
constexpr int L_O = L_Q + 384;
constexpr int L_X = L_O + 4 * RS_O;
constexpr int L_RED = L_X + 4 * RS_X;
constexpr int L_ML = L_RED + 4096 + 512;
constexpr int L_FLAG = L_ML + 128;
constexpr int L_END = L_FLAG + 256;
constexpr int F_LN = 0, F_RED = 4, F_Q = 8, F_SCALE = 9, F_WO = 16, F_ML = 24, F_P1 = 32, F_P2 = 40;

__device__ __forceinline__ float bf_lo(unsigned u) { return __uint_as_float(u << 16); }
__device__ __forceinline__ float bf_hi(unsigned u) { return __uint_as_float(u & 0xffff0000u); }
__device__ __forceinline__ unsigned cvtpk(float lo, float hi) {
  bf16x2 r = {(__bf16)lo, (__bf16)hi};
  return __builtin_bit_cast(unsigned, r);
}
__device__ __forceinline__ float rbf(float x) { return bf_lo(cvtpk(x, 0.f)); }
__device__ __forceinline__ float dot2(unsigned k, unsigned q, float acc) {
  return __builtin_amdgcn_fdot2_f32_bf16(__builtin_bit_cast(bf16x2, k), __builtin_bit_cast(bf16x2, q), acc, false);
}
template <int CTRL> __device__ __forceinline__ unsigned dppu(unsigned x) { return (unsigned)__builtin_amdgcn_mov_dpp((int)x, CTRL, 0xf, 0xf, true); }
template <int CTRL> __device__ __forceinline__ float dpp(float x) { return __uint_as_float(dppu<CTRL>(__float_as_uint(x))); }
constexpr int XOR1 = 0xB1, XOR2 = 0x4E, XOR7 = 0x141, XOR8 = 0x128;
__device__ __forceinline__ float swap16_add(float a, float b) {
  auto s = __builtin_amdgcn_permlane16_swap(__float_as_uint(a), __float_as_uint(b), false, false);
  return __uint_as_float(s[0]) + __uint_as_float(s[1]);
}
__device__ __forceinline__ float swap32_add(float a, float b) {
  auto s = __builtin_amdgcn_permlane32_swap(__float_as_uint(a), __float_as_uint(b), false, false);
  return __uint_as_float(s[0]) + __uint_as_float(s[1]);
}
__device__ __forceinline__ float x16_sum(float x) { x = swap16_add(x, x); return swap32_add(x, x); }
__device__ __forceinline__ float x16_max(float x) {
  auto s = __builtin_amdgcn_permlane16_swap(__float_as_uint(x), __float_as_uint(x), false, false);
  x = fmaxf(__uint_as_float(s[0]), __uint_as_float(s[1]));
  auto t = __builtin_amdgcn_permlane32_swap(__float_as_uint(x), __float_as_uint(x), false, false);
  return fmaxf(__uint_as_float(t[0]), __uint_as_float(t[1]));
}
__device__ __forceinline__ float wave_sum(float x) {
  x += dpp<XOR1>(x); x += dpp<XOR2>(x); x += dpp<XOR7>(x); x += dpp<XOR8>(x);
  return x16_sum(x);
}
__device__ __forceinline__ void bar() { asm volatile("s_waitcnt lgkmcnt(0)\n\ts_barrier" ::: "memory"); }
template <int N> __device__ __forceinline__ void wait_vm() { asm volatile("s_waitcnt vmcnt(%0)" ::"n"(N) : "memory"); }
__device__ __forceinline__ rsrc_t mk_rsrc(const void* p, unsigned bytes) {
  return __builtin_amdgcn_make_buffer_rsrc((void*)p, 0, bytes, 0x00020000);
}
constexpr int AUX_NT = 2, AUX_SC1 = 16;
template <int AUX = AUX_SC1> __device__ __forceinline__ void put2(rsrc_t rs, unsigned off, unsigned a, unsigned b, unsigned tag) {
  __builtin_amdgcn_raw_buffer_store_b128(u32x4{a, tag, b, tag}, rs, off, 0, AUX);
}
template <int AUX> __device__ __forceinline__ u32x4 get2(rsrc_t rs, unsigned off) { return __builtin_amdgcn_raw_buffer_load_b128(rs, off, 0, AUX); }
__device__ __forceinline__ bool fresh(u32x4 a, unsigned tag) { return (a[1] == tag) & (a[3] == tag); }
template <int N, int SLEEP, int AUX = AUX_SC1> __device__ __forceinline__ void gather(rsrc_t rs, unsigned off, unsigned S, unsigned tag, u32x4 (&a)[N]) {
  bool ok = false;
#pragma unroll 1
  for (unsigned spins = 0; spins < SPIN_CAP; ++spins) {
    asm volatile("" ::: "memory");
    if (!ok) {
#pragma unroll
      for (int i = 0; i < N; ++i) a[i] = get2<AUX>(rs, off + i * S);
      ok = true;
#pragma unroll
      for (int i = 0; i < N; ++i) ok &= fresh(a[i], tag);
    }
    if (__all(ok)) break;
    __builtin_amdgcn_s_sleep(SLEEP);
  }
}
__device__ __forceinline__ s16x4 tr_read(unsigned char* lds, int off) {
  return __builtin_amdgcn_ds_read_tr16_b64_v4i16((LAS s16x4*)(lds + off));
}
__device__ __forceinline__ f32x4 mfma32(s16x4 a0, s16x4 a1, s16x4 b0, s16x4 b1, f32x4 c) {
  const s16x8 a = __builtin_shufflevector(a0, a1, 0, 1, 2, 3, 4, 5, 6, 7), b = __builtin_shufflevector(b0, b1, 0, 1, 2, 3, 4, 5, 6, 7);
  return __builtin_amdgcn_mfma_f32_16x16x32_bf16(__builtin_bit_cast(bf16x8, a), __builtin_bit_cast(bf16x8, b), c, 0, 0, 0);
}
__device__ __forceinline__ unsigned lds_addr(unsigned char* lds, int off) { return (unsigned)(size_t)(LAS unsigned char*)lds + off; }
__device__ __forceinline__ void lds_set(unsigned char* lds, int flag) {
  asm volatile("s_waitcnt lgkmcnt(0)\n\tds_write_b32 %0, %1" ::"v"(lds_addr(lds, L_FLAG + 4 * flag)), "v"(1u) : "memory");
}
__device__ __forceinline__ void lds_wait(unsigned char* lds, int flag) {
  unsigned t, n = SPIN_CAP << 4;
  asm volatile("1:\n\tds_read_b32 %0, %2\n\ts_waitcnt lgkmcnt(0)\n\tv_cmp_eq_u32 vcc, 0, %0\n\ts_cbranch_vccz 2f\n\t"
               "s_sleep 1\n\ts_sub_u32 %1, %1, 1\n\ts_cbranch_scc0 1b\n2:"
               : "=&v"(t), "+s"(n) : "v"(lds_addr(lds, L_FLAG + 4 * flag)) : "vcc", "scc", "memory");
}
__device__ __forceinline__ void dma16(const void* base, unsigned ldsaddr, int voff) {
  const unsigned long long b = (unsigned long long)base;
  const u32x4 rs = {(unsigned)b, (unsigned)(b >> 32) & 0xffffu, 0x7fffffffu, 0x00020000u};
  unsigned keep;
  asm volatile("s_mov_b32 %0, m0\n\ts_mov_b32 m0, %1\n\ts_nop 4\n\tbuffer_load_dwordx4 %2, %3, 0 offen nt lds\n\ts_mov_b32 m0, %0"
               : "=&s"(keep) : "s"(ldsaddr), "v"(voff), "s"(rs) : "memory");
}
__device__ __forceinline__ void wait_until(unsigned t) {
  asm volatile("1:\n\ts_memrealtime vcc\n\ts_waitcnt lgkmcnt(0)\n\ts_sub_u32 vcc_lo, vcc_lo, %0\n\ts_cmp_lt_i32 vcc_lo, 0\n\t"
               "s_cbranch_scc0 2f\n\ts_sleep 1\n\ts_branch 1b\n2:" ::"s"(t) : "vcc", "scc");
}
__device__ __forceinline__ unsigned next_tag(unsigned epoch_v) { return __builtin_amdgcn_readfirstlane(epoch_v) + 1u; }
template <int I> struct ic { static constexpr int value = I; };
template <int I, int N, class F> __device__ __forceinline__ void static_for(F&& f) {
  if constexpr (I < N) { f(ic<I>{}); static_for<I + 1, N>(f); }
}
__host__ __device__ constexpr int issued(int G) {
  int a = G < 0 ? RING : RING + (G >= 1 ? (G + CATCH - 1) / CATCH : 0), b = G + 1 + LEAD, n = a > b ? a : b;
  return n > 32 ? 32 : n;
}

#define ST(i) do {} while (0)
#define ST_END() do {} while (0)
#define PST(i) do {} while (0)

struct Params {
  const u16 *x, *kc, *vc, *wq, *wo, *wg, *wu, *wd;
  u16* out;
  unsigned char* ws;
};

__global__ void __launch_bounds__(NTHR, 4) fused_decode(Params p) {
  __shared__ __attribute__((aligned(16))) unsigned char lds[L_END];
  const int tid = threadIdx.x, lane = tid & 63, w = __builtin_amdgcn_readfirstlane(tid >> 6);
  const int wg = blockIdx.x, idx = wg >> 3, h = (wg & 7) * 4 + (idx >> 3), j = idx & 7;
  const int g4 = lane >> 4, i16 = lane & 15, bl = (lane >> 2) & 3;
  const int tr_row = 4 * g4 + bl, tr_col = 8 * (lane & 3);
  const rsrc_t rws = mk_rsrc(p.ws, WS_END);
  const unsigned epoch_v = __hip_atomic_load((GAS unsigned*)p.ws, __ATOMIC_RELAXED, __HIP_MEMORY_SCOPE_AGENT);
  unsigned tag;
  unsigned stampv = 0;

  ST(0);
  const unsigned tstart = TS ? (unsigned)__builtin_amdgcn_s_memrealtime() : 0u;
  const int f = (idx >> 3) * 2 + j - 6, nt = j < 6 ? 2 : f < 6 ? 4 : 0;
  unsigned xv[5];
  if (w >= 4) {
    const u16* bo = p.wo + (size_t)DH * h * DM + 80 * j;
    const u16* bd = p.wd + (size_t)48 * h * DM + 80 * j - 1920 / 10 * DM;
    if (w == 4) *((LAS unsigned*)(lds + L_FLAG) + lane) = 0u;
    bar();
    ST(20);
#pragma unroll 1
    for (int pc = w > 4 ? w - 5 : 38; pc < 38; pc += 8) {
      const int ci = pc * 64 + lane, row = (ci * 6554) >> 16;
      if (XP & 1) { if (pc < 37 || lane < 32) dma16(pc < 30 ? p.wo + (size_t)(h * 8 + j) * 15360 : p.wd + (size_t)(h * 8 + j) * 3840 - 15360, lds_addr(lds, L_WO) + pc * 1024, ci * 16); } else
      if (pc < 37 || lane < 32) dma16(pc < 30 ? bo : bd, lds_addr(lds, L_WO) + pc * 1024, row * (DM * 2 - 160) + ci * 16);
    }
  } else {
    const u16* xr = p.x + w * DM;
    asm volatile("global_load_dword %0, %5, %6\n\tglobal_load_dword %1, %5, %6 offset:256\n\tglobal_load_dword %2, %5, %6 offset:512\n\t"
                 "global_load_dword %3, %5, %6 offset:768\n\tglobal_load_dword %4, %5, %6 offset:1024"
                 : "=&v"(xv[0]), "=&v"(xv[1]), "=&v"(xv[2]), "=&v"(xv[3]), "=&v"(xv[4]) : "v"(lane * 4), "s"(xr) : "memory");
    const u16* bw = j < 6 ? p.wq + (size_t)160 * w * QN + h * DH + 32 * j
                          : (f < 3 ? p.wg : p.wu) + (size_t)160 * w * FF + 192 * (wg & 7) + 64 * (f < 3 ? f : f - 3);
    const int sh = j < 6 ? 2 : 3; int gs = (j < 6 ? QN * 2 : FF * 2) << (6 - sh);
    int vo = (lane >> sh) * (j < 6 ? QN * 2 : FF * 2) + (lane & ((1 << sh) - 1)) * 16;
    if ((XP & 2) && j < 6) {
      bw = p.wq + (size_t)(320 * (j & 1) + 80 * w) * QN + h * DH + 64 * (j >> 1);
      vo = (lane >> 3) * QN * 2 + (lane & 7) * 16; gs = QN * 2 * 8;
    }
#pragma unroll 1
    for (int i = 0; i < 5 * nt; ++i, vo += gs) dma16(bw, lds_addr(lds, L_W1) + (w * 5 * nt + i) * 1024, vo);
    bar();
  }
  int wr = w;
  asm volatile("" : "+s"(wr));
  s16x4 wf[15];

  if (wr > 4) {
    ST(18);
    const int sw = w - 5, r = lane >> 3, c = lane & 7;
    const size_t kvoff = ((size_t)((j >> 1) * NH + h) * SK + (SK - WIN) + (j & 1) * 1024) * DH;
    const rsrc_t rk = mk_rsrc(p.kc + kvoff, 1024 * DH * 2), rv = mk_rsrc(p.vc + kvoff, 1024 * DH * 2);
    const int voff = r * 384 + c * 16;
    u32x4 ring[RING][3];
    const unsigned hs = ((unsigned)(ROTM == 3 ? wg : wg * 8 + sw) * 2654435761u) >> 16;
    const int rot = ROTM == 0 ? idx : hs >> 3, rmul = ROTM == 2 || ROTM == 3 ? (hs & 7) * 2 + 1 : 1;
#define LOAD(G, jj) ((G) < 16 ? __builtin_amdgcn_raw_buffer_load_b128(rk, voff + (jj) * 128, ((((G) * rmul + rot) & 15) * 8 + sw) * 3072, KAUX) \
                           : __builtin_amdgcn_raw_buffer_load_b128(rv, voff + (jj) * 128, ((((G) * rmul + rot) & 15) * 8 + sw) * 3072, VAUX))
    __builtin_amdgcn_sched_barrier(0);
    if (PRIO && sw >= 4) __builtin_amdgcn_s_setprio(1);
    wait_vm<WQW>();
    ST(1);
    static_for<0, 3 * RING>([&](auto nc) {
      constexpr int N = decltype(nc)::value;
      if constexpr (N >= FLY) wait_vm<FLY - 1>();
      if constexpr (N >= FLY && (N - FLY) % 3 == 2) PST((N - FLY) / 3);
      if constexpr (N == FLY) {
        lds_set(lds, F_WO + sw);
        ST(21);
        ST(22);
      }
      if constexpr (N == QPOLL) lds_set(lds, F_P1 + sw);
      ring[N / 3][N % 3] = LOAD(N / 3, N % 3);
      __builtin_amdgcn_sched_barrier(0);
    });
    ST(3);
    lds_wait(lds, F_Q);
    ST(4);
    tag = next_tag(epoch_v);
    asm volatile("" : "+s"(tag));
    u32x4 qv[3];
#pragma unroll
    for (int jj = 0; jj < 3; ++jj) qv[jj] = *(LAS u32x4*)(lds + L_Q + jj * 128 + c * 16);
#define STEP(G)                                                                                         \
  do {                                                                                                  \
    __builtin_amdgcn_sched_barrier(0);                                                                  \
    if constexpr (PRIO && (G) == PRIO) __builtin_amdgcn_s_setprio(0);                                   \
    if constexpr (STAMP == 2 && (G) >= (3 * RING - FLY) / 3) { wait_vm<3 * (issued((G) - 1) - (G) - 1)>(); PST(G); } \
    if constexpr (issued(G) > issued((G) - 1)) {                                                        \
      wait_vm<3 * (issued((G) - 1) - (G) - 1)>();                                                       \
      if (TS) wait_until(tstart + TA + issued((G) - 1) * TS);                                           \
      _Pragma("unroll") for (int N = 3 * issued((G) - 1); N < 3 * issued(G); ++N) ring[(N / 3) % RING][N % 3] = LOAD(N / 3, N % 3); \
    }                                                                                                   \
    __builtin_amdgcn_sched_barrier(0);                                                                  \
  } while (0)
    float sc[16];
    static_for<0, 16>([&](auto gc) {
      constexpr int G = decltype(gc)::value;
      STEP(G);
      float d0 = 0.f, d1 = 0.f, d2 = 0.f;
#pragma unroll
      for (int k = 0; k < 4; ++k) {
        d0 = dot2(ring[G % RING][0][k], qv[0][k], d0);
        d1 = dot2(ring[G % RING][1][k], qv[1][k], d1);
        d2 = dot2(ring[G % RING][2][k], qv[2][k], d2);
      }
      float d = (d0 + d1) + d2;
      d += dpp<XOR1>(d); d += dpp<XOR2>(d); d += dpp<XOR7>(d);
      sc[G] = rbf(rbf(d) * 0.07216878364870322f);
      asm volatile("" : "+v"(sc[G]));
    });
    ST(5);
    float m = sc[0];
#pragma unroll
    for (int G = 1; G < 16; ++G) m = fmaxf(m, sc[G]);
    m = fmaxf(m, dpp<XOR8>(m));
    m = x16_max(m);
    float l = 0.f;
#pragma unroll
    for (int G = 0; G < 16; ++G) { sc[G] = __builtin_amdgcn_exp2f((sc[G] - m) * LOG2E); l += sc[G]; }
    l += dpp<XOR8>(l);
    l = x16_sum(l);
    *(LAS f32x2*)(lds + L_ML + sw * 8) = f32x2{m, l};
    lds_set(lds, F_ML + sw);
    f32x2 acc[12];
    float scale;
#pragma unroll
    for (int k = 0; k < 12; ++k) acc[k] = f32x2{0.f, 0.f};
    static_for<16, 32>([&](auto gc) {
      constexpr int G = decltype(gc)::value;
      STEP(G);
      if constexpr (G == LPOLL) lds_set(lds, F_P2 + sw);
      if constexpr (G == 30) {
        lds_wait(lds, F_SCALE);
        const f32x2 ml = *(LAS f32x2*)(lds + L_ML + 64);
        scale = __builtin_amdgcn_exp2f((m - ml[0]) * LOG2E) * ml[1];
      }
      const f32x2 pp = {sc[G - 16], sc[G - 16]};
#pragma unroll
      for (int jj = 0; jj < 3; ++jj)
#pragma unroll
        for (int k = 0; k < 4; ++k) {
          const unsigned u = ring[G % RING][jj][k];
          acc[jj * 4 + k] = __builtin_elementwise_fma(f32x2{bf_lo(u), bf_hi(u)}, pp, acc[jj * 4 + k]);
        }
    });
#undef STEP
#undef LOAD
    ST(6);
    ST(17);
    float t12[12], t6[6];
#pragma unroll
    for (int k = 0; k < 12; ++k) t12[k] = swap32_add(acc[k >> 1][k & 1], acc[(k + 12) >> 1][k & 1]);
#pragma unroll
    for (int k = 0; k < 6; ++k) t6[k] = swap16_add(t12[k], t12[k + 6]);
    LAS float* mrg = (LAS float*)(lds + L_MRG) + sw * DH;
#pragma unroll
    for (int k = 0; k < 3; ++k) {
      const float a = t6[k] + dpp<XOR8>(t6[k]), b2 = t6[k + 3] + dpp<XOR8>(t6[k + 3]);
      const int e = 3 * r + k;
      mrg[(e >> 3) * 64 + c * 8 + (e & 7)] = ((lane & 8) ? b2 : a) * scale;
    }
  } else {
    __builtin_amdgcn_s_setprio(3);
    if (w == 4) {
      tag = next_tag(epoch_v);
      const int l47 = min(lane, 47), bb = l47 / 12;
      {
        u32x4 a[1];
        lds_wait(lds, F_P1);
        gather<1, SLQ>(rws, WS_QP + (h * 4 + (j >> 1)) * 768 + l47 * 16, 0, tag, a);
        *(LAS u32x2*)(lds + L_Q + l47 * 8) = u32x2{a[0][0], a[0][2]};
        lds_set(lds, F_Q);
        ST(8);
      }
      lds_wait(lds, F_ML + (lane & 7));
      const f32x2 ml = *(LAS f32x2*)(lds + L_ML + (lane & 7) * 8);
      float M = fmaxf(ml[0], dpp<XOR1>(ml[0]));
      M = fmaxf(M, dpp<XOR2>(M)); M = fmaxf(M, dpp<XOR7>(M));
      float L = __builtin_amdgcn_exp2f((ml[0] - M) * LOG2E) * ml[1];
      L += dpp<XOR1>(L); L += dpp<XOR2>(L); L += dpp<XOR7>(L);
      if (lane == 0) put2(rws, WS_ML + (h * 8 + j) * 128, __float_as_uint(M), __float_as_uint(L), tag);
      {
        u32x4 a[2];
        gather<2, 32>(rws, WS_GU + (((wg & 7) * 8 + bb) * 96 + 24 * (h & 3)) * 8 + (l47 - bb * 12) * 16, 4 * 768, tag, a);
        float hv[4];
#pragma unroll
        for (int e = 0; e < 4; ++e) {
          const float gg = (e & 1) ? bf_hi(a[0][e & 2]) : bf_lo(a[0][e & 2]), uu = (e & 1) ? bf_hi(a[1][e & 2]) : bf_lo(a[1][e & 2]);
          const float inner = 0.7978845608028654f * (gg + 0.044715f * gg * gg * gg);
          hv[e] = rbf(0.5f * gg * (1.f + tanhf(inner))) * uu;
        }
        *(LAS u32x2*)(lds + L_O + bb * RS_O + DH * 2 + (l47 - bb * 12) * 8) = u32x2{cvtpk(hv[0], hv[1]), cvtpk(hv[2], hv[3])};
        ST(10);
      }
      {
        u32x4 a[1];
        lds_wait(lds, F_P2);
        gather<1, 16>(rws, WS_ML + (h * 8 + (j ^ 1)) * 128, 0, tag, a);
        const float M2 = __uint_as_float(a[0][0]), MM = fmaxf(M, M2);
        const float e1 = __builtin_amdgcn_exp2f((M - MM) * LOG2E) * L, e2 = __builtin_amdgcn_exp2f((M2 - MM) * LOG2E) * __uint_as_float(a[0][2]);
        *(LAS f32x2*)(lds + L_ML + 64) = f32x2{MM, __builtin_amdgcn_rcpf(e1 + e2)};
        lds_set(lds, F_SCALE);
        ST(11);
      }
    } else {
      if (nt == 0) wait_vm<0>();
      asm volatile("s_waitcnt vmcnt(10)" : "+v"(xv[0]), "+v"(xv[1]), "+v"(xv[2]), "+v"(xv[3]), "+v"(xv[4]));
      float s1 = 0.f;
#pragma unroll
      for (int s = 0; s < 5; ++s) s1 += bf_lo(xv[s]) + bf_hi(xv[s]);
      const float mean = wave_sum(s1) * (1.f / DM);
      float s2 = 0.f;
#pragma unroll
      for (int s = 0; s < 5; ++s) {
        const float a = bf_lo(xv[s]) - mean, c = bf_hi(xv[s]) - mean;
        s2 = fmaf(a, a, s2); s2 = fmaf(c, c, s2);
      }
      const float mb = rbf(mean), rs = __builtin_amdgcn_rcpf(rbf(sqrtf(rbf(rbf(wave_sum(s2) * (1.f / DM)) + 1e-5f))));
#pragma unroll
      for (int s = 0; s < 5; ++s)
        *(LAS unsigned*)(lds + L_X + w * RS_X + 256 * s + 4 * lane) = cvtpk(rbf(bf_lo(xv[s]) - mb) * rs, rbf(bf_hi(xv[s]) - mb) * rs);
      lds_set(lds, F_LN + w);
      ST(7);
      lds_wait(lds, F_LN + (lane & 3));
      s16x4 af[10];
#pragma unroll
      for (int s = 0; s < 10; ++s) af[s] = *(LAS s16x4*)(lds + L_X + bl * RS_X + (160 * w + 16 * s + 4 * g4) * 2);
      const int rsb = 32 * nt, a0 = L_W1 + (160 * w + tr_row) * rsb + tr_col;
      wait_vm<0>();
      tag = next_tag(epoch_v);
      ST(9);
#pragma unroll 1
      for (int t = 0; t < nt; ++t) {
        f32x4 d = {0.f, 0.f, 0.f, 0.f};
#pragma unroll
        for (int s = 0; s < 10; s += 2)
          d = mfma32(af[s], af[s + 1], tr_read(lds, a0 + 16 * s * rsb + 32 * t), tr_read(lds, a0 + 16 * (s + 1) * rsb + 32 * t), d);
        *(LAS float*)(lds + L_RED + ((w * 4 + t) * 64 + lane) * 4) = d[0];
      }
      lds_set(lds, F_RED + w);
      if (w == 0) {
        lds_wait(lds, F_RED + (lane & 3));
#pragma unroll 1
        for (int t = 0; t < nt; ++t) {
          const LAS float* r = (const LAS float*)(lds + L_RED) + t * 64 + lane;
          const float v = (r[0] + r[256]) + (r[512] + r[768]);
          *(LAS u16*)(lds + L_RED + 4096 + (g4 * 16 * nt + 16 * t + i16) * 2) = (u16)cvtpk(v, v);
        }
        const u32x2 pk = *(LAS u32x2*)(lds + L_RED + 4096 + lane * 8);
        const unsigned po = j < 6 ? WS_QP + ((h * 4 + (lane >> 3)) * 96 + 16 * j) * 8 + (lane & 7) * 16
                                  : WS_GU + ((((wg & 7) * 2 + (f < 3 ? 0 : 1)) * 4 + g4) * 96 + 32 * (f < 3 ? f : f - 3)) * 8 + i16 * 16;
        if (lane < 16 * nt) put2(rws, po, pk[0], pk[1], tag);
        ST(2);
      }
    }
    lds_wait(lds, F_WO + (lane & 7));
#pragma unroll
    for (int s = 0; s < 15; ++s) wf[s] = tr_read(lds, L_WO + (16 * s + tr_row) * RS_WO + 32 * w + tr_col);
  }

  bar();
  ST(12);
  if (w == 5) {
    const int l47 = min(lane, 47);
    f32x4 o = *(LAS f32x4*)(lds + L_MRG + l47 * 16);
#pragma unroll
    for (int i = 1; i < 8; ++i) o += *(LAS f32x4*)(lds + L_MRG + i * 768 + l47 * 16);
    if (lane < 48) put2<S1A>(rws, WS_OP + (h * 8 + j) * 768 + lane * 16, cvtpk(o[0], o[1]), cvtpk(o[2], o[3]), tag);
    ST(13);
  } else if (w >= 6 && w < 9) {
    const int t = tid - 6 * 64, bb = t / 48, q4 = t - bb * 48;
    u32x4 a[2];
    gather<2, SL1, L1A>(rws, WS_OP + (h * 8 + 2 * bb) * 768 + q4 * 16, 768, tag, a);
    *(LAS u32x2*)(lds + L_O + bb * RS_O + q4 * 8) = u32x2{cvtpk(bf_lo(a[0][0]) + bf_lo(a[1][0]), bf_hi(a[0][0]) + bf_hi(a[1][0])),
                                                          cvtpk(bf_lo(a[0][2]) + bf_lo(a[1][2]), bf_hi(a[0][2]) + bf_hi(a[1][2]))};
    ST(14);
  }
  bar();
  if (w < 5) {
    s16x4 of[15];
#pragma unroll
    for (int s = 0; s < 15; ++s) of[s] = *(LAS s16x4*)(lds + L_O + bl * RS_O + (16 * s + 4 * g4) * 2);
    f32x4 d0 = {0.f, 0.f, 0.f, 0.f}, d1 = d0;
#pragma unroll
    for (int s = 0; s < 8; s += 2) d0 = mfma32(of[s], of[s + 1], wf[s], wf[s + 1], d0);
#pragma unroll
    for (int s = 8; s < 14; s += 2) d1 = mfma32(of[s], of[s + 1], wf[s], wf[s + 1], d1);
    d1 = mfma32(of[14], s16x4{0, 0, 0, 0}, wf[14], wf[14], d1);
    const float y = d0[0] + d1[0];
    const unsigned pk = cvtpk(y, dpp<XOR1>(y)), pk2 = dppu<XOR2>(pk);
    if (!(lane & 3)) put2<S2A>(rws, WS_YP + ((j * 10 + 2 * w + (g4 >> 1)) * 32 + h) * 128 + (g4 & 1) * 64 + i16 * 4, pk, pk2, tag);
    ST(15);
  } else if (w == 9 && h < 10) {
    const int oi = (2 * (h & 1) + ((lane >> 2) & 1)) * DM + 80 * j + 16 * (h >> 1) + 4 * (lane & 3);
    const u32x2 xr = *(const u32x2*)(p.x + oi);
    u32x4 a[4];
    gather<4, SL2, L2A>(rws, WS_YP + (j * 10 + h) * 4096 + lane * 16, 1024, tag, a);
    float s[4];
#pragma unroll
    for (int e = 0; e < 4; ++e) {
      float v[4];
#pragma unroll
      for (int i = 0; i < 4; ++i) v[i] = (e & 1) ? bf_hi(a[i][e & 2]) : bf_lo(a[i][e & 2]);
      s[e] = (v[0] + v[1]) + (v[2] + v[3]);
      s[e] += dpp<XOR8>(s[e]);
      s[e] = x16_sum(s[e]);
    }
    if (lane < 8) *(u32x2*)(p.out + oi) = u32x2{cvtpk(bf_lo(xr[0]) + s[0], bf_hi(xr[0]) + s[1]), cvtpk(bf_lo(xr[1]) + s[2], bf_hi(xr[1]) + s[3])};
    if (wg == 0 && lane == 0) __hip_atomic_store((GAS unsigned*)p.ws, tag, __ATOMIC_RELAXED, __HIP_MEMORY_SCOPE_AGENT);
    ST(16);
  }
  ST_END();
}

extern "C" void kernel_launch(void* const* d_in, const int* in_sizes, int n_in, void* d_out, int out_size, void* d_ws,
                              size_t ws_size, hipStream_t stream) {
  Params p;
  p.x = (const u16*)d_in[0];
  p.kc = (const u16*)d_in[1];
  p.vc = (const u16*)d_in[2];
  p.wq = (const u16*)d_in[3];
  p.wo = (const u16*)d_in[4];
  p.wg = (const u16*)d_in[6];
  p.wu = (const u16*)d_in[7];
  p.wd = (const u16*)d_in[8];
  p.out = (u16*)d_out;
  p.ws = (unsigned char*)d_ws;
  fused_decode<<<256, NTHR, 0, stream>>>(p);
}
```

```cpp
#include <hip/hip_runtime.h>
#include <hip/hip_bf16.h>
#include <stdint.h>

#define RING 8
#define FLY 5
#define LEAD 2
#define CATCH 4
#define WQW 4
#define CODELEN 16000
#define SL1 1
#define SL2 0
#define SLQ 16
#define SLQ0 64
#define TS 87
#define TA 300
#define L1A 16
#define L2A 16
#define S1A 16
#define S2A 16
#define XP 0
#define ROTM 0
#define SWAP 1
#define SLQN 1
#define QPOLL 15
#define LPOLL 24
#define PRIO 0
#define KAUX 2
#define VAUX 2
#define PFP 0
#define STG 0
#define D1 0
#define D2 24
#define ROTE 8 * idx
#define STAMP 0

typedef unsigned short u16;
typedef unsigned u32x2 __attribute__((ext_vector_type(2)));
typedef unsigned u32x4 __attribute__((ext_vector_type(4)));
typedef float f32x2 __attribute__((ext_vector_type(2)));
typedef float f32x4 __attribute__((ext_vector_type(4)));
typedef short s16x4 __attribute__((ext_vector_type(4)));
typedef short s16x8 __attribute__((ext_vector_type(8)));
typedef __bf16 bf16x2 __attribute__((ext_vector_type(2)));
typedef __bf16 bf16x8 __attribute__((ext_vector_type(8)));
#define LAS __attribute__((address_space(3)))
#define GAS __attribute__((address_space(1)))
typedef __amdgpu_buffer_rsrc_t rsrc_t;

constexpr int DM = 640, NH = 32, DH = 192, SK = 8192, WIN = 2048, FF = 1536, QN = NH * DH;
constexpr int NTHR = 832;
constexpr float LOG2E = 1.4426950408889634f;
constexpr unsigned SPIN_CAP = 1u << 18;

constexpr unsigned WS_QP = 4096;
constexpr unsigned WS_GU = WS_QP + 32 * 4 * 96 * 8;
constexpr unsigned WS_ML = WS_GU + 8 * 2 * 4 * 96 * 8;
constexpr unsigned WS_OP = WS_ML + 32 * 8 * 128;
constexpr unsigned WS_YP = WS_OP + 32 * 8 * 96 * 8;
constexpr unsigned WS_END = WS_YP + 8 * 10 * 32 * 128;

constexpr int RS_WO = 160, RS_O = 496, RS_X = 1296;
constexpr int L_W1 = 0;
constexpr int L_WO = L_W1 + 640 * 128;
constexpr int L_MRG = L_WO + 240 * RS_WO;
constexpr int L_Q = L_MRG + 8 * 768;
constexpr int L_O = L_Q + 384;
constexpr int L_X = L_O + 4 * RS_O;
constexpr int L_RED = L_X + 4 * RS_X;
constexpr int L_ML = L_RED + 4096 + 512;
constexpr int L_FLAG = L_ML + 128;
constexpr int L_END = L_FLAG + 256;
constexpr int F_LN = 0, F_RED = 4, F_Q = 8, F_SCALE = 9, F_WO = 16, F_ML = 24, F_P1 = 32, F_P2 = 40;

__device__ __forceinline__ float bf_lo(unsigned u) { return __uint_as_float(u << 16); }
__device__ __forceinline__ float bf_hi(unsigned u) { return __uint_as_float(u & 0xffff0000u); }
__device__ __forceinline__ unsigned cvtpk(float lo, float hi) {
  bf16x2 r = {(__bf16)lo, (__bf16)hi};
  return __builtin_bit_cast(unsigned, r);
}
__device__ __forceinline__ float rbf(float x) { return bf_lo(cvtpk(x, 0.f)); }
__device__ __forceinline__ float dot2(unsigned k, unsigned q, float acc) {
  return __builtin_amdgcn_fdot2_f32_bf16(__builtin_bit_cast(bf16x2, k), __builtin_bit_cast(bf16x2, q), acc, false);
}
template <int CTRL> __device__ __forceinline__ unsigned dppu(unsigned x) { return (unsigned)__builtin_amdgcn_mov_dpp((int)x, CTRL, 0xf, 0xf, true); }
template <int CTRL> __device__ __forceinline__ float dpp(float x) { return __uint_as_float(dppu<CTRL>(__float_as_uint(x))); }
constexpr int XOR1 = 0xB1, XOR2 = 0x4E, XOR7 = 0x141, XOR8 = 0x128;
__device__ __forceinline__ float swap16_add(float a, float b) {
  auto s = __builtin_amdgcn_permlane16_swap(__float_as_uint(a), __float_as_uint(b), false, false);
  return __uint_as_float(s[0]) + __uint_as_float(s[1]);
}
__device__ __forceinline__ float swap32_add(float a, float b) {
  auto s = __builtin_amdgcn_permlane32_swap(__float_as_uint(a), __float_as_uint(b), false, false);
  return __uint_as_float(s[0]) + __uint_as_float(s[1]);
}
__device__ __forceinline__ float x16_sum(float x) { x = swap16_add(x, x); return swap32_add(x, x); }
__device__ __forceinline__ float x16_max(float x) {
  auto s = __builtin_amdgcn_permlane16_swap(__float_as_uint(x), __float_as_uint(x), false, false);
  x = fmaxf(__uint_as_float(s[0]), __uint_as_float(s[1]));
  auto t = __builtin_amdgcn_permlane32_swap(__float_as_uint(x), __float_as_uint(x), false, false);
  return fmaxf(__uint_as_float(t[0]), __uint_as_float(t[1]));
}
__device__ __forceinline__ float wave_sum(float x) {
  x += dpp<XOR1>(x); x += dpp<XOR2>(x); x += dpp<XOR7>(x); x += dpp<XOR8>(x);
  return x16_sum(x);
}
__device__ __forceinline__ void bar() { asm volatile("s_waitcnt lgkmcnt(0)\n\ts_barrier" ::: "memory"); }
template <int N> __device__ __forceinline__ void wait_vm() { asm volatile("s_waitcnt vmcnt(%0)" ::"n"(N) : "memory"); }
__device__ __forceinline__ rsrc_t mk_rsrc(const void* p, unsigned bytes) {
  return __builtin_amdgcn_make_buffer_rsrc((void*)p, 0, bytes, 0x00020000);
}
constexpr int AUX_NT = 2, AUX_SC1 = 16;
template <int AUX = AUX_SC1> __device__ __forceinline__ void put2(rsrc_t rs, unsigned off, unsigned a, unsigned b, unsigned tag) {
  __builtin_amdgcn_raw_buffer_store_b128(u32x4{a, tag, b, tag}, rs, off, 0, AUX);
}
template <int AUX> __device__ __forceinline__ u32x4 get2(rsrc_t rs, unsigned off) { return __builtin_amdgcn_raw_buffer_load_b128(rs, off, 0, AUX); }
__device__ __forceinline__ bool fresh(u32x4 a, unsigned tag) { return (a[1] == tag) & (a[3] == tag); }
template <int N, int SLEEP, int AUX = AUX_SC1> __device__ __forceinline__ void gather(rsrc_t rs, unsigned off, unsigned S, unsigned tag, u32x4 (&a)[N]) {
  bool ok = false;
#pragma unroll 1
  for (unsigned spins = 0; spins < SPIN_CAP; ++spins) {
    asm volatile("" ::: "memory");
    if (!ok) {
#pragma unroll
      for (int i = 0; i < N; ++i) a[i] = get2<AUX>(rs, off + i * S);
      ok = true;
#pragma unroll
      for (int i = 0; i < N; ++i) ok &= fresh(a[i], tag);
    }
    if (__all(ok)) break;
    __builtin_amdgcn_s_sleep(SLEEP);
  }
}
__device__ __forceinline__ s16x4 tr_read(unsigned char* lds, int off) {
  return __builtin_amdgcn_ds_read_tr16_b64_v4i16((LAS s16x4*)(lds + off));
}
__device__ __forceinline__ f32x4 mfma32(s16x4 a0, s16x4 a1, s16x4 b0, s16x4 b1, f32x4 c) {
  const s16x8 a = __builtin_shufflevector(a0, a1, 0, 1, 2, 3, 4, 5, 6, 7), b = __builtin_shufflevector(b0, b1, 0, 1, 2, 3, 4, 5, 6, 7);
  return __builtin_amdgcn_mfma_f32_16x16x32_bf16(__builtin_bit_cast(bf16x8, a), __builtin_bit_cast(bf16x8, b), c, 0, 0, 0);
}
__device__ __forceinline__ unsigned lds_addr(unsigned char* lds, int off) { return (unsigned)(size_t)(LAS unsigned char*)lds + off; }
__device__ __forceinline__ void lds_set(unsigned char* lds, int flag) {
  asm volatile("s_waitcnt lgkmcnt(0)\n\tds_write_b32 %0, %1" ::"v"(lds_addr(lds, L_FLAG + 4 * flag)), "v"(1u) : "memory");
}
__device__ __forceinline__ void lds_wait(unsigned char* lds, int flag) {
  unsigned t, n = SPIN_CAP << 4;
  asm volatile("1:\n\tds_read_b32 %0, %2\n\ts_waitcnt lgkmcnt(0)\n\tv_cmp_eq_u32 vcc, 0, %0\n\ts_cbranch_vccz 2f\n\t"
               "s_sleep 1\n\ts_sub_u32 %1, %1, 1\n\ts_cbranch_scc0 1b\n2:"
               : "=&v"(t), "+s"(n) : "v"(lds_addr(lds, L_FLAG + 4 * flag)) : "vcc", "scc", "memory");
}
__device__ __forceinline__ void dma16(const void* base, unsigned ldsaddr, int voff) {
  const unsigned long long b = (unsigned long long)base;
  const u32x4 rs = {(unsigned)b, (unsigned)(b >> 32) & 0xffffu, 0x7fffffffu, 0x00020000u};
  unsigned keep;
  asm volatile("s_mov_b32 %0, m0\n\ts_mov_b32 m0, %1\n\ts_nop 4\n\tbuffer_load_dwordx4 %2, %3, 0 offen nt lds\n\ts_mov_b32 m0, %0"
               : "=&s"(keep) : "s"(ldsaddr), "v"(voff), "s"(rs) : "memory");
}
__device__ __forceinline__ void wait_until(unsigned t) {
  asm volatile("1:\n\ts_memrealtime vcc\n\ts_waitcnt lgkmcnt(0)\n\ts_sub_u32 vcc_lo, vcc_lo, %0\n\ts_cmp_lt_i32 vcc_lo, 0\n\t"
               "s_cbranch_scc0 2f\n\ts_sleep 1\n\ts_branch 1b\n2:" ::"s"(t) : "vcc", "scc");
}
__device__ __forceinline__ unsigned next_tag(unsigned epoch_v) { return __builtin_amdgcn_readfirstlane(epoch_v) + 1u; }
template <int I> struct ic { static constexpr int value = I; };
template <int I, int N, class F> __device__ __forceinline__ void static_for(F&& f) {
  if constexpr (I < N) { f(ic<I>{}); static_for<I + 1, N>(f); }
}
__host__ __device__ constexpr int issued(int G) {
  int a = G < 0 ? RING : RING + (G >= 1 ? (G + CATCH - 1) / CATCH : 0), b = G + 1 + LEAD, n = a > b ? a : b;
  return n > 32 ? 32 : n;
}

#define ST(i) do {} while (0)
#define ST_END() do {} while (0)
#define PST(i) do {} while (0)

struct Params {
  const u16 *x, *kc, *vc, *wq, *wo, *wg, *wu, *wd;
  u16* out;
  unsigned char* ws;
};

__global__ void __launch_bounds__(NTHR, 4) fused_decode(Params p) {
  __shared__ __attribute__((aligned(16))) unsigned char lds[L_END];
  const int tid = threadIdx.x, lane = tid & 63, w = __builtin_amdgcn_readfirstlane(tid >> 6);
  const int wg = blockIdx.x, idx = wg >> 3, h = (wg & 7) * 4 + (idx >> 3), j = idx & 7;
  const int g4 = lane >> 4, i16 = lane & 15, bl = (lane >> 2) & 3;
  const int tr_row = 4 * g4 + bl, tr_col = 8 * (lane & 3);
  const rsrc_t rws = mk_rsrc(p.ws, WS_END);
  const unsigned epoch_v = __hip_atomic_load((GAS unsigned*)p.ws, __ATOMIC_RELAXED, __HIP_MEMORY_SCOPE_AGENT);
  unsigned tag;
  unsigned stampv = 0;

  ST(0);
  const unsigned tstart = TS ? (unsigned)__builtin_amdgcn_s_memrealtime() : 0u;
  const int f = (idx >> 3) * 2 + j - 6, nt = j < 6 ? 2 : f < 6 ? 4 : 0;
  unsigned xv[5];
  if (w >= 4) {
    const u16* bo = p.wo + (size_t)DH * h * DM + 80 * j;
    const u16* bd = p.wd + (size_t)48 * h * DM + 80 * j - 1920 / 10 * DM;
    if (w == 4) *((LAS unsigned*)(lds + L_FLAG) + lane) = 0u;
    bar();
    ST(20);
#pragma unroll 1
    for (int pc = w > 4 ? w - 5 : 38; pc < 38; pc += 8) {
      const int ci = pc * 64 + lane, row = (ci * 6554) >> 16;
      if (XP & 1) { if (pc < 37 || lane < 32) dma16(pc < 30 ? p.wo + (size_t)(h * 8 + j) * 15360 : p.wd + (size_t)(h * 8 + j) * 3840 - 15360, lds_addr(lds, L_WO) + pc * 1024, ci * 16); } else
      if (pc < 37 || lane < 32) dma16(pc < 30 ? bo : bd, lds_addr(lds, L_WO) + pc * 1024, row * (DM * 2 - 160) + ci * 16);
    }
  } else {
    const u16* xr = p.x + w * DM;
    asm volatile("global_load_dword %0, %5, %6\n\tglobal_load_dword %1, %5, %6 offset:256\n\tglobal_load_dword %2, %5, %6 offset:512\n\t"
                 "global_load_dword %3, %5, %6 offset:768\n\tglobal_load_dword %4, %5, %6 offset:1024"
                 : "=&v"(xv[0]), "=&v"(xv[1]), "=&v"(xv[2]), "=&v"(xv[3]), "=&v"(xv[4]) : "v"(lane * 4), "s"(xr) : "memory");
    const u16* bw = j < 6 ? p.wq + (size_t)160 * w * QN + h * DH + 32 * j
                          : (f < 3 ? p.wg : p.wu) + (size_t)160 * w * FF + 192 * (wg & 7) + 64 * (f < 3 ? f : f - 3);
    const int sh = j < 6 ? 2 : 3; int gs = (j < 6 ? QN * 2 : FF * 2) << (6 - sh);
    int vo = (lane >> sh) * (j < 6 ? QN * 2 : FF * 2) + (lane & ((1 << sh) - 1)) * 16;
    if ((XP & 2) && j < 6) {
      bw = p.wq + (size_t)(320 * (j & 1) + 80 * w) * QN + h * DH + 64 * (j >> 1);
      vo = (lane >> 3) * QN * 2 + (lane & 7) * 16; gs = QN * 2 * 8;
    }
#pragma unroll 1
    for (int i = 0; i < 5 * nt; ++i, vo += gs) dma16(bw, lds_addr(lds, L_W1) + (w * 5 * nt + i) * 1024, vo);
    bar();
  }
  int wr = w;
  asm volatile("" : "+s"(wr));
  s16x4 wf[15];

  if (wr > 4) {
    ST(18);
    const int sw = w - 5, r = lane >> 3, c = lane & 7;
    const size_t kvoff = ((size_t)((j >> 1) * NH + h) * SK + (SK - WIN) + (j & 1) * 1024) * DH;
    const rsrc_t rk = mk_rsrc(p.kc + kvoff, 1024 * DH * 2), rv = mk_rsrc(p.vc + kvoff, 1024 * DH * 2);
    const int voff = r * 384 + c * 16;
    u32x4 ring[RING][3];
    const int xc = wg & 7;
    const int rot = sw + (ROTE);
#define LOAD(G, jj) ((G) < 16 ? __builtin_amdgcn_raw_buffer_load_b128(rk, voff + (jj) * 128, (((G) * 8 + rot) & 127) * 3072, KAUX) \
                           : __builtin_amdgcn_raw_buffer_load_b128(rv, voff + (jj) * 128, (((G) * 8 + rot) & 127) * 3072, VAUX))
    __builtin_amdgcn_sched_barrier(0);
    const unsigned tpace = tstart + TA + (STG == 1 ? sw * TS / 8 : STG == 2 ? (idx & 7) * TS / 8 : STG == 3 ? (sw & 1) * TS / 2 : STG == 4 ? (wg & 7) * TS / 8 : 0);
    if (PRIO && sw >= 4) __builtin_amdgcn_s_setprio(1);
    wait_vm<WQW>();
    ST(1);
    static_for<0, 3 * RING>([&](auto nc) {
      constexpr int N = decltype(nc)::value;
      if constexpr (N >= FLY) wait_vm<FLY - 1>();
      if constexpr (N >= FLY && (N - FLY) % 3 == 2) PST((N - FLY) / 3);
      if constexpr (N == FLY) {
        lds_set(lds, F_WO + sw);
        ST(21);
        ST(22);
      }
      if constexpr (N == QPOLL) lds_set(lds, F_P1 + sw);
      if constexpr (TS && PFP && N % 3 == 0 && N >= 3) wait_until(tpace + (N / 3) * TS);
      ring[N / 3][N % 3] = LOAD(N / 3, N % 3);
      __builtin_amdgcn_sched_barrier(0);
    });
    ST(3);
    lds_wait(lds, F_Q);
    ST(4);
    tag = next_tag(epoch_v);
    asm volatile("" : "+s"(tag));
    u32x4 qv[3];
#pragma unroll
    for (int jj = 0; jj < 3; ++jj) qv[jj] = *(LAS u32x4*)(lds + L_Q + jj * 128 + c * 16);
#define STEP(G)                                                                                         \
  do {                                                                                                  \
    __builtin_amdgcn_sched_barrier(0);                                                                  \
    if constexpr (PRIO && (G) == PRIO) __builtin_amdgcn_s_setprio(0);                                   \
    if constexpr (STAMP == 2 && (G) >= (3 * RING - FLY) / 3) { wait_vm<3 * (issued((G) - 1) - (G) - 1)>(); PST(G); } \
    if constexpr (issued(G) > issued((G) - 1)) {                                                        \
      wait_vm<3 * (issued((G) - 1) - (G) - 1)>();                                                       \
      if (TS) wait_until(tpace + issued((G) - 1) * TS);                                                 \
      _Pragma("unroll") for (int N = 3 * issued((G) - 1); N < 3 * issued(G); ++N) ring[(N / 3) % RING][N % 3] = LOAD(N / 3, N % 3); \
    }                                                                                                   \
    __builtin_amdgcn_sched_barrier(0);                                                                  \
  } while (0)
    float sc[16];
    static_for<0, 16>([&](auto gc) {
      constexpr int G = decltype(gc)::value;
      STEP(G);
      float d0 = 0.f, d1 = 0.f, d2 = 0.f;
#pragma unroll
      for (int k = 0; k < 4; ++k) {
        d0 = dot2(ring[G % RING][0][k], qv[0][k], d0);
        d1 = dot2(ring[G % RING][1][k], qv[1][k], d1);
        d2 = dot2(ring[G % RING][2][k], qv[2][k], d2);
      }
      float d = (d0 + d1) + d2;
      d += dpp<XOR1>(d); d += dpp<XOR2>(d); d += dpp<XOR7>(d);
      sc[G] = rbf(rbf(d) * 0.07216878364870322f);
      asm volatile("" : "+v"(sc[G]));
    });
    ST(5);
    float m = sc[0];
#pragma unroll
    for (int G = 1; G < 16; ++G) m = fmaxf(m, sc[G]);
    m = fmaxf(m, dpp<XOR8>(m));
    m = x16_max(m);
    float l = 0.f;
#pragma unroll
    for (int G = 0; G < 16; ++G) { sc[G] = __builtin_amdgcn_exp2f((sc[G] - m) * LOG2E); l += sc[G]; }
    l += dpp<XOR8>(l);
    l = x16_sum(l);
    *(LAS f32x2*)(lds + L_ML + sw * 8) = f32x2{m, l};
    lds_set(lds, F_ML + sw);
    f32x2 acc[12];
    float scale;
#pragma unroll
    for (int k = 0; k < 12; ++k) acc[k] = f32x2{0.f, 0.f};
    static_for<16, 32>([&](auto gc) {
      constexpr int G = decltype(gc)::value;
      STEP(G);
      if constexpr (G == LPOLL) lds_set(lds, F_P2 + sw);
      if constexpr (G == 30) {
        lds_wait(lds, F_SCALE);
        const f32x2 ml = *(LAS f32x2*)(lds + L_ML + 64);
        scale = __builtin_amdgcn_exp2f((m - ml[0]) * LOG2E) * ml[1];
      }
      const f32x2 pp = {sc[G - 16], sc[G - 16]};
#pragma unroll
      for (int jj = 0; jj < 3; ++jj)
#pragma unroll
        for (int k = 0; k < 4; ++k) {
          const unsigned u = ring[G % RING][jj][k];
          acc[jj * 4 + k] = __builtin_elementwise_fma(f32x2{bf_lo(u), bf_hi(u)}, pp, acc[jj * 4 + k]);
        }
    });
#undef STEP
#undef LOAD
    ST(6);
    ST(17);
    float t12[12], t6[6];
#pragma unroll
    for (int k = 0; k < 12; ++k) t12[k] = swap32_add(acc[k >> 1][k & 1], acc[(k + 12) >> 1][k & 1]);
#pragma unroll
    for (int k = 0; k < 6; ++k) t6[k] = swap16_add(t12[k], t12[k + 6]);
    LAS float* mrg = (LAS float*)(lds + L_MRG) + sw * DH;
#pragma unroll
    for (int k = 0; k < 3; ++k) {
      const float a = t6[k] + dpp<XOR8>(t6[k]), b2 = t6[k + 3] + dpp<XOR8>(t6[k + 3]);
      const int e = 3 * r + k;
      mrg[(e >> 3) * 64 + c * 8 + (e & 7)] = ((lane & 8) ? b2 : a) * scale;
    }
  } else {
    __builtin_amdgcn_s_setprio(3);
    if (w == 4) {
      tag = next_tag(epoch_v);
      const int l47 = min(lane, 47), bb = l47 / 12;
      {
        u32x4 a[1];
        lds_wait(lds, F_P1);
        gather<1, SLQ>(rws, WS_QP + (h * 4 + (j >> 1)) * 768 + l47 * 16, 0, tag, a);
        *(LAS u32x2*)(lds + L_Q + l47 * 8) = u32x2{a[0][0], a[0][2]};
        lds_set(lds, F_Q);
        ST(8);
      }
      lds_wait(lds, F_ML + (lane & 7));
      const f32x2 ml = *(LAS f32x2*)(lds + L_ML + (lane & 7) * 8);
      float M = fmaxf(ml[0], dpp<XOR1>(ml[0]));
      M = fmaxf(M, dpp<XOR2>(M)); M = fmaxf(M, dpp<XOR7>(M));
      float L = __builtin_amdgcn_exp2f((ml[0] - M) * LOG2E) * ml[1];
      L += dpp<XOR1>(L); L += dpp<XOR2>(L); L += dpp<XOR7>(L);
      if (lane == 0) put2(rws, WS_ML + (h * 8 + j) * 128, __float_as_uint(M), __float_as_uint(L), tag);
      {
        u32x4 a[2];
        gather<2, 32>(rws, WS_GU + (((wg & 7) * 8 + bb) * 96 + 24 * (h & 3)) * 8 + (l47 - bb * 12) * 16, 4 * 768, tag, a);
        float hv[4];
#pragma unroll
        for (int e = 0; e < 4; ++e) {
          const float gg = (e & 1) ? bf_hi(a[0][e & 2]) : bf_lo(a[0][e & 2]), uu = (e & 1) ? bf_hi(a[1][e & 2]) : bf_lo(a[1][e & 2]);
          const float inner = 0.7978845608028654f * (gg + 0.044715f * gg * gg * gg);
          hv[e] = rbf(0.5f * gg * (1.f + tanhf(inner))) * uu;
        }
        *(LAS u32x2*)(lds + L_O + bb * RS_O + DH * 2 + (l47 - bb * 12) * 8) = u32x2{cvtpk(hv[0], hv[1]), cvtpk(hv[2], hv[3])};
        ST(10);
      }
      {
        u32x4 a[1];
        lds_wait(lds, F_P2);
        gather<1, 16>(rws, WS_ML + (h * 8 + (j ^ 1)) * 128, 0, tag, a);
        const float M2 = __uint_as_float(a[0][0]), MM = fmaxf(M, M2);
        const float e1 = __builtin_amdgcn_exp2f((M - MM) * LOG2E) * L, e2 = __builtin_amdgcn_exp2f((M2 - MM) * LOG2E) * __uint_as_float(a[0][2]);
        *(LAS f32x2*)(lds + L_ML + 64) = f32x2{MM, __builtin_amdgcn_rcpf(e1 + e2)};
        lds_set(lds, F_SCALE);
        ST(11);
      }
    } else {
      if (nt == 0) wait_vm<0>();
      asm volatile("s_waitcnt vmcnt(10)" : "+v"(xv[0]), "+v"(xv[1]), "+v"(xv[2]), "+v"(xv[3]), "+v"(xv[4]));
      float s1 = 0.f;
#pragma unroll
      for (int s = 0; s < 5; ++s) s1 += bf_lo(xv[s]) + bf_hi(xv[s]);
      const float mean = wave_sum(s1) * (1.f / DM);
      float s2 = 0.f;
#pragma unroll
      for (int s = 0; s < 5; ++s) {
        const float a = bf_lo(xv[s]) - mean, c = bf_hi(xv[s]) - mean;
        s2 = fmaf(a, a, s2); s2 = fmaf(c, c, s2);
      }
      const float mb = rbf(mean), rs = __builtin_amdgcn_rcpf(rbf(sqrtf(rbf(rbf(wave_sum(s2) * (1.f / DM)) + 1e-5f))));
#pragma unroll
      for (int s = 0; s < 5; ++s)
        *(LAS unsigned*)(lds + L_X + w * RS_X + 256 * s + 4 * lane) = cvtpk(rbf(bf_lo(xv[s]) - mb) * rs, rbf(bf_hi(xv[s]) - mb) * rs);
      lds_set(lds, F_LN + w);
      ST(7);
      lds_wait(lds, F_LN + (lane & 3));
      s16x4 af[10];
#pragma unroll
      for (int s = 0; s < 10; ++s) af[s] = *(LAS s16x4*)(lds + L_X + bl * RS_X + (160 * w + 16 * s + 4 * g4) * 2);
      const int rsb = 32 * nt, a0 = L_W1 + (160 * w + tr_row) * rsb + tr_col;
      wait_vm<0>();
      tag = next_tag(epoch_v);
      ST(9);
#pragma unroll 1
      for (int t = 0; t < nt; ++t) {
        f32x4 d = {0.f, 0.f, 0.f, 0.f};
#pragma unroll
        for (int s = 0; s < 10; s += 2)
          d = mfma32(af[s], af[s + 1], tr_read(lds, a0 + 16 * s * rsb + 32 * t), tr_read(lds, a0 + 16 * (s + 1) * rsb + 32 * t), d);
        *(LAS float*)(lds + L_RED + ((w * 4 + t) * 64 + lane) * 4) = d[0];
      }
      lds_set(lds, F_RED + w);
      if (w == 0) {
        lds_wait(lds, F_RED + (lane & 3));
#pragma unroll 1
        for (int t = 0; t < nt; ++t) {
          const LAS float* r = (const LAS float*)(lds + L_RED) + t * 64 + lane;
          const float v = (r[0] + r[256]) + (r[512] + r[768]);
          *(LAS u16*)(lds + L_RED + 4096 + (g4 * 16 * nt + 16 * t + i16) * 2) = (u16)cvtpk(v, v);
        }
        const u32x2 pk = *(LAS u32x2*)(lds + L_RED + 4096 + lane * 8);
        const unsigned po = j < 6 ? WS_QP + ((h * 4 + (lane >> 3)) * 96 + 16 * j) * 8 + (lane & 7) * 16
                                  : WS_GU + ((((wg & 7) * 2 + (f < 3 ? 0 : 1)) * 4 + g4) * 96 + 32 * (f < 3 ? f : f - 3)) * 8 + i16 * 16;
        if (lane < 16 * nt) put2(rws, po, pk[0], pk[1], tag);
        ST(2);
      }
    }
    lds_wait(lds, F_WO + (lane & 7));
#pragma unroll
    for (int s = 0; s < 15; ++s) wf[s] = tr_read(lds, L_WO + (16 * s + tr_row) * RS_WO + 32 * w + tr_col);
  }

  bar();
  ST(12);
  if (w == 5) {
    const int l47 = min(lane, 47);
    f32x4 o = *(LAS f32x4*)(lds + L_MRG + l47 * 16);
#pragma unroll
    for (int i = 1; i < 8; ++i) o += *(LAS f32x4*)(lds + L_MRG + i * 768 + l47 * 16);
    if (lane < 48) put2<S1A>(rws, WS_OP + (h * 8 + j) * 768 + lane * 16, cvtpk(o[0], o[1]), cvtpk(o[2], o[3]), tag);
    ST(13);
  } else if (w >= 6 && w < 9) {
    const int t = tid - 6 * 64, bb = t / 48, q4 = t - bb * 48;
    u32x4 a[2];
    if (D1) __builtin_amdgcn_s_sleep(D1);
    gather<2, SL1, L1A>(rws, WS_OP + (h * 8 + 2 * bb) * 768 + q4 * 16, 768, tag, a);
    *(LAS u32x2*)(lds + L_O + bb * RS_O + q4 * 8) = u32x2{cvtpk(bf_lo(a[0][0]) + bf_lo(a[1][0]), bf_hi(a[0][0]) + bf_hi(a[1][0])),
                                                          cvtpk(bf_lo(a[0][2]) + bf_lo(a[1][2]), bf_hi(a[0][2]) + bf_hi(a[1][2]))};
    ST(14);
  }
  bar();
  if (w < 5) {
    s16x4 of[15];
#pragma unroll
    for (int s = 0; s < 15; ++s) of[s] = *(LAS s16x4*)(lds + L_O + bl * RS_O + (16 * s + 4 * g4) * 2);
    f32x4 d0 = {0.f, 0.f, 0.f, 0.f}, d1 = d0;
#pragma unroll
    for (int s = 0; s < 8; s += 2) d0 = mfma32(of[s], of[s + 1], wf[s], wf[s + 1], d0);
#pragma unroll
    for (int s = 8; s < 14; s += 2) d1 = mfma32(of[s], of[s + 1], wf[s], wf[s + 1], d1);
    d1 = mfma32(of[14], s16x4{0, 0, 0, 0}, wf[14], wf[14], d1);
    const float y = d0[0] + d1[0];
    const unsigned pk = cvtpk(y, dpp<XOR1>(y)), pk2 = dppu<XOR2>(pk);
    if (!(lane & 3)) put2<S2A>(rws, WS_YP + ((j * 10 + 2 * w + (g4 >> 1)) * 32 + h) * 128 + (g4 & 1) * 64 + i16 * 4, pk, pk2, tag);
    ST(15);
  } else if (w == 9 && h < 10) {
    const int oi = (2 * (h & 1) + ((lane >> 2) & 1)) * DM + 80 * j + 16 * (h >> 1) + 4 * (lane & 3);
    const u32x2 xr = *(const u32x2*)(p.x + oi);
    u32x4 a[4];
    if (D2) __builtin_amdgcn_s_sleep(D2);
    gather<4, SL2, L2A>(rws, WS_YP + (j * 10 + h) * 4096 + lane * 16, 1024, tag, a);
    float s[4];
#pragma unroll
    for (int e = 0; e < 4; ++e) {
      float v[4];
#pragma unroll
      for (int i = 0; i < 4; ++i) v[i] = (e & 1) ? bf_hi(a[i][e & 2]) : bf_lo(a[i][e & 2]);
      s[e] = (v[0] + v[1]) + (v[2] + v[3]);
      s[e] += dpp<XOR8>(s[e]);
      s[e] = x16_sum(s[e]);
    }
    if (lane < 8) *(u32x2*)(p.out + oi) = u32x2{cvtpk(bf_lo(xr[0]) + s[0], bf_hi(xr[0]) + s[1]), cvtpk(bf_lo(xr[1]) + s[2], bf_hi(xr[1]) + s[3])};
    if (wg == 0 && lane == 0) __hip_atomic_store((GAS unsigned*)p.ws, tag, __ATOMIC_RELAXED, __HIP_MEMORY_SCOPE_AGENT);
    ST(16);
  }
  ST_END();
}

extern "C" void kernel_launch(void* const* d_in, const int* in_sizes, int n_in, void* d_out, int out_size, void* d_ws,
                              size_t ws_size, hipStream_t stream) {
  Params p;
  p.x = (const u16*)d_in[0];
  p.kc = (const u16*)d_in[1];
  p.vc = (const u16*)d_in[2];
  p.wq = (const u16*)d_in[3];
  p.wo = (const u16*)d_in[4];
  p.wg = (const u16*)d_in[6];
  p.wu = (const u16*)d_in[7];
  p.wd = (const u16*)d_in[8];
  p.out = (u16*)d_out;
  p.ws = (unsigned char*)d_ws;
  fused_decode<<<256, NTHR, 0, stream>>>(p);
}
```
